```python
import torch

def _softmax(x, axis=-1):
    return torch.softmax(x, dim=axis)

def _gelu(x):
    return torch.nn.functional.gelu(x, approximate='tanh')

def _silu(x):
    return torch.nn.functional.silu(x)

def _sigmoid(x):
    return torch.sigmoid(x)

def _layernorm(x, axis=-1, eps=1e-05):
    m = x.mean(dim=axis, keepdim=True)
    v = x.var(dim=axis, keepdim=True, unbiased=False)
    return (x - m) / torch.sqrt(v + eps)

def _scatter_add(tgt, idx, src, axis=0):
    return tgt.clone().index_add_(axis, idx, src)

def _segment_sum(src, seg, n):
    out = torch.zeros((n,) + src.shape[1:], dtype=src.dtype)
    return out.index_add_(0, seg, src)

def _pad(x, pw):
    flat = tuple((v for pair in reversed(pw) for v in pair))
    return torch.nn.functional.pad(x, flat)
kernel_name = 'gated_mix__memory_aug_recurrent_mem_tr__linear_attn_block__prompt'

def setup_inputs():

    _w = lambda a: (lambda t: t.to(torch.bfloat16) if t.is_floating_point() else t)(torch.as_tensor(a))

    def _A_setup():
        B, S, Dm = (1, 2048, 576)
        H, Dh = (32, 128)
        M, seg = (8, 32)
        seg = min(seg, S)
        S = S // seg * seg
        return {'x': _w((lambda *s: torch.randn(*s, dtype=torch.bfloat16) * 0.05)(B, S, Dm)), 'mem0': _w((lambda *s: torch.randn(*s, dtype=torch.bfloat16) * 0.8)(B, M, Dm)), 'Wq': _w((lambda *s: torch.randn(*s, dtype=torch.bfloat16) * 3.2)(Dm, H, Dh) * Dm ** (-0.5)), 'Wk': _w((lambda *s: torch.randn(*s, dtype=torch.bfloat16) * 3.2)(Dm, H, Dh) * Dm ** (-0.5)), 'Wv': _w((lambda *s: torch.randn(*s, dtype=torch.bfloat16) * 0.8)(Dm, H, Dh) * Dm ** (-0.5)), 'Wo': _w((lambda *s: torch.randn(*s, dtype=torch.bfloat16) * 0.8)(H, Dh, Dm) * (H * Dh) ** (-0.5)), 'seg_len': seg}
    _A_in = _A_setup()

    def _B_setup():
        B, S, D = (1, 2048, 576)
        return {'x': _w((lambda *s: torch.randn(*s, dtype=torch.bfloat16) * 0.05)(B, S, D)), 'wqkv': _w((lambda *s: torch.randn(*s, dtype=torch.bfloat16) * 0.8)(D, 3 * D)), 'wo': _w((lambda *s: torch.randn(*s, dtype=torch.bfloat16) * 0.2)(D, D))}
    _B_in = _B_setup()
    return {'A_x': _A_in['x'], 'A_mem0': _A_in['mem0'], 'A_Wq': _A_in['Wq'], 'A_Wk': _A_in['Wk'], 'A_Wv': _A_in['Wv'], 'A_Wo': _A_in['Wo'], 'A_seg_len': _A_in['seg_len'], 'B_x': _B_in['x'], 'B_wqkv': _B_in['wqkv'], 'B_wo': _B_in['wo'], 'gate_w': _w((lambda *s: torch.randn(*s, dtype=torch.bfloat16) * 0.05)(576) * 16.0)}

def reference(A_x, A_mem0, A_Wq, A_Wk, A_Wv, A_Wo, A_seg_len, B_x, B_wqkv, B_wo, gate_w):

    def _A_ref(x, mem0, Wq, Wk, Wv, Wo, seg_len):
        B, S, Dm = x.shape
        H, Dh = (Wq.shape[1], Wq.shape[2])
        M = mem0.shape[1]
        scale = Dh ** (-0.5)
        n_seg = S // seg_len
        L = M + seg_len
        idx = torch.arange(L)
        mask = (idx[:, None] < M) | (idx[:, None] >= idx[None, :])
        mem = mem0
        outs = []
        for s in range(n_seg):
            xs = x[:, s * seg_len:(s + 1) * seg_len, :].clone()
            h = torch.concatenate([mem, xs], axis=1)
            q = torch.einsum('btd,dhe->bhte', h, Wq)
            k = torch.einsum('btd,dhe->bhte', h, Wk)
            v = torch.einsum('btd,dhe->bhte', h, Wv)
            scores = torch.einsum('bhqe,bhke->bhqk', q, k) * scale
            scores = torch.where(mask[None, None], scores, -1000000000.0)
            p = _softmax(scores, axis=-1)
            o = torch.einsum('bhqk,bhke->bhqe', p, v)
            o = torch.einsum('bhte,hed->btd', o, Wo)
            mem = o[:, :M, :].clone()
            outs.append(o[:, M:, :])
        return torch.concatenate(outs, axis=1)

    def _B_ref(x, wqkv, wo):
        B, S, D = x.shape
        H = 32
        Dh = D // H
        qkv = (lambda x, *d: x.permute(*d))((x @ wqkv).reshape(B, S, 3, H, Dh), 2, 0, 3, 1, 4)
        q, k, v = (qkv[0], qkv[1], qkv[2])
        phi = lambda t: torch.where(t > 0, t + 1, torch.exp(t)) + 1e-06
        pq, pk = (phi(q), phi(k))
        kv = torch.einsum('bhsd,bhse->bhde', pk, v)
        z = torch.einsum('bhsd->bhd', pk)
        num = torch.einsum('bhqd,bhde->bhqe', pq, kv)
        den = torch.einsum('bhqd,bhd->bhq', pq, z)[..., None] + 1e-06
        o = num / den
        return (lambda x, *d: x.permute(*d))(o, 0, 2, 1, 3).reshape(B, S, D) @ wo
    x = A_x
    g = _sigmoid((x * gate_w).sum(-1, keepdims=True))
    a = _A_ref(x, A_mem0, A_Wq, A_Wk, A_Wv, A_Wo, A_seg_len)
    b = _B_ref(x, B_wqkv, B_wo)
    return g * a + (1 - g) * b
```

```cpp
#include <hip/hip_runtime.h>
#include <hip/hip_bf16.h>
#include <stdint.h>

#define GAS __attribute__((address_space(1)))
#define LAS __attribute__((address_space(3)))
typedef unsigned v4u __attribute__((ext_vector_type(4)));
typedef unsigned v2u __attribute__((ext_vector_type(2)));
typedef float f32x4 __attribute__((ext_vector_type(4)));
typedef __bf16 bf16x8 __attribute__((ext_vector_type(8)));
typedef __bf16 bf16x2 __attribute__((ext_vector_type(2)));
typedef short s16x4 __attribute__((ext_vector_type(4)));
typedef GAS unsigned gu32;
typedef __amdgpu_buffer_rsrc_t rsrc_t;
#define RLX_AGENT __ATOMIC_RELAXED, __HIP_MEMORY_SCOPE_AGENT

constexpr unsigned MEMOFF = 131072u;
constexpr unsigned MBASE = 1179648u;
constexpr unsigned VRBASE = 1572864u;
constexpr unsigned OFF_CTRL = 2097152u;
constexpr unsigned OFF_KF = 4194304u;
constexpr unsigned OFF_VF = 20971520u;
constexpr unsigned OFF_KM = 37748736u;
constexpr unsigned OFF_VM = 46137344u;
constexpr unsigned C_KVF = 196608u;
constexpr unsigned C_KMC = 65536u;
constexpr unsigned C_TMO = 64u;
constexpr unsigned C_PASS = 16384u;
constexpr unsigned OFF_HS = 3145728u;
constexpr unsigned HS_CH = 4096u;
constexpr unsigned HS_EC = 8192u;
constexpr unsigned COMM_BYTES = 2097152u;
constexpr unsigned LDS_BYTES = 163840u;
constexpr unsigned OFF_OF = 0x3400000u;
constexpr unsigned OFF_WP = 0x4400000u;
constexpr unsigned OFF_B1P = 0x6800000u;
constexpr unsigned OFF_NF = 0x6B00000u;
constexpr unsigned OFF_BB = 0x6C00000u;
constexpr unsigned OFF_XF = 0x7000000u;
constexpr unsigned OFF_G = 0x7300000u;
constexpr unsigned C_PRB = 0x200u;
constexpr unsigned C_SEL = 0x180u;
constexpr unsigned C_B1 = 0x80u;
constexpr unsigned C_B15 = 0x100u;
constexpr unsigned C_BBF = 0x58000u;
constexpr unsigned CTRL_URGENT = 0x50000u;
constexpr unsigned C_XFL = 0x1000u;
constexpr unsigned C_WFL = 0x70000u;
constexpr unsigned C_OFL = 0x50000u;
constexpr unsigned CTRL_BYTES = 0xB0000u;
constexpr unsigned WS_BYTES = 0x7800000u;

#define FAST0 61
struct Params {
    const unsigned short* x; const unsigned short* mem0; const unsigned short* wq; const unsigned short* wk; const unsigned short* wv; const unsigned short* wo;
    const unsigned short* wqkv; const unsigned short* wob; const unsigned short* gw; unsigned short* out; unsigned char* ws;
    unsigned wsbytes; unsigned mask;
};

__device__ __forceinline__ rsrc_t mk_rsrc(const void* p, unsigned bytes) { return __builtin_amdgcn_make_buffer_rsrc((void*)p, 0, bytes, 0x00020000); }
__device__ __forceinline__ v4u ld_sc1(rsrc_t r, unsigned off) { return __builtin_amdgcn_raw_buffer_load_b128(r, off, 0, 16); }
__device__ __forceinline__ void st_sc1(rsrc_t r, unsigned off, v4u v) { __builtin_amdgcn_raw_buffer_store_b128(v, r, off, 0, 16); }
__device__ __forceinline__ unsigned pk2(float a, float b) { bf16x2 v; v[0] = (__bf16)a; v[1] = (__bf16)b; return __builtin_bit_cast(unsigned, v); }
__device__ __forceinline__ f32x4 mfma32(v4u a, v4u b, f32x4 c) { return __builtin_amdgcn_mfma_f32_16x16x32_bf16(__builtin_bit_cast(bf16x8, a), __builtin_bit_cast(bf16x8, b), c, 0, 0, 0); }
__device__ __forceinline__ f32x4 mfma16(v2u a, v2u b, f32x4 c) { return __builtin_amdgcn_mfma_f32_16x16x16bf16_1k(__builtin_bit_cast(s16x4, a), __builtin_bit_cast(s16x4, b), c, 0, 0, 0); }
__device__ __forceinline__ v2u trread(unsigned char* lds, unsigned off) { s16x4 v = __builtin_amdgcn_ds_read_tr16_b64_v4i16((LAS s16x4*)((LAS unsigned char*)lds + off)); return __builtin_bit_cast(v2u, v); }
__device__ __forceinline__ int fresh_lane() { int l; asm volatile("v_mbcnt_lo_u32_b32 %0, -1, 0\n\tv_mbcnt_hi_u32_b32 %0, -1, %0" : "=v"(l)); return l; }
#define TL(slot, idx) do { } while (0)
#define DRAIN() asm volatile("s_waitcnt vmcnt(0)" ::: "memory")
__device__ __forceinline__ void set_tmo(unsigned char* ws) { __hip_atomic_store((gu32*)(ws + OFF_CTRL + C_TMO), 1u, RLX_AGENT); }
__device__ __forceinline__ bool wait_ge(unsigned char* ws, unsigned off, unsigned want) {
    gu32* w = (gu32*)(ws + off);
    for (unsigned spins = 0;; ++spins) {
        if ((unsigned)__builtin_amdgcn_readfirstlane(__hip_atomic_load(w, RLX_AGENT)) >= want) break;
        __builtin_amdgcn_s_sleep(2);
        if (spins > (1u << 21)) { set_tmo(ws); return false; }
        if ((spins & 1023u) == 1023u && __builtin_amdgcn_readfirstlane(__hip_atomic_load((gu32*)(ws + OFF_CTRL + C_TMO), RLX_AGENT))) return false;
    }
    __builtin_amdgcn_fence(__ATOMIC_ACQUIRE, "wavefront");
    return true;
}

__device__ __forceinline__ bool wait_nz4(unsigned char* ws, rsrc_t rw, unsigned off) {
    for (unsigned spins = 0;; ++spins) {
        asm volatile("" ::: "memory");
        const v4u v = ld_sc1(rw, off);
        if (__builtin_amdgcn_readfirstlane(min(min(v.x, v.y), min(v.z, v.w)))) break;
        __builtin_amdgcn_s_sleep(2);
        if (spins > (1u << 21)) { set_tmo(ws); return false; }
        if ((spins & 1023u) == 1023u && __builtin_amdgcn_readfirstlane(__hip_atomic_load((gu32*)(ws + OFF_CTRL + C_TMO), RLX_AGENT))) return false;
    }
    __builtin_amdgcn_fence(__ATOMIC_ACQUIRE, "wavefront");
    return true;
}

__device__ __forceinline__ bool wait_nz8(unsigned char* ws, unsigned off, int lane) {
    gu32* w = (gu32*)(ws + off) + (lane & 7);
    for (unsigned spins = 0;; ++spins) {
        if (__all(__hip_atomic_load(w, RLX_AGENT) != 0u)) break;
        __builtin_amdgcn_s_sleep(4);
        if (spins > (1u << 21)) { set_tmo(ws); return false; }
        if ((spins & 1023u) == 1023u && __builtin_amdgcn_readfirstlane(__hip_atomic_load((gu32*)(ws + OFF_CTRL + C_TMO), RLX_AGENT))) return false;
    }
    __builtin_amdgcn_fence(__ATOMIC_ACQUIRE, "wavefront");
    return true;
}
__device__ __forceinline__ bool wait_fin(unsigned char* ws, unsigned off, unsigned off2, int lane, bool slow) {
    gu32* w = lane < 16 ? (gu32*)(ws + off) + (lane & 1) + (lane >> 1) * 16 : (gu32*)(ws + off2);
    for (unsigned spins = 0;; ++spins) {
        if (__all(__hip_atomic_load(w, RLX_AGENT) != 0u)) break;
        __builtin_amdgcn_s_sleep(2);
        if (slow) __builtin_amdgcn_s_sleep(12);
        if (spins > (1u << 21)) { set_tmo(ws); return false; }
        if ((spins & 1023u) == 1023u && __builtin_amdgcn_readfirstlane(__hip_atomic_load((gu32*)(ws + OFF_CTRL + C_TMO), RLX_AGENT))) return false;
    }
    __builtin_amdgcn_fence(__ATOMIC_ACQUIRE, "wavefront");
    return true;
}

__device__ __forceinline__ float bf2f(unsigned short v) { return __uint_as_float((unsigned)v << 16); }
__device__ __forceinline__ float rbf(float v) { return (float)(__bf16)v; }
__device__ __forceinline__ int colset(int ks, int eta, int g, int j) { return ks < 8 ? 144 * (ks >> 1) + 16 * j + 8 * (ks & 1) + 2 * g + eta : 144 * g + 128 + 8 * eta + j; }
constexpr unsigned HS_R = 0x3000u, HS_B = 0u, HS_X = 0x4000u;
__device__ __forceinline__ v2u hs_peek(unsigned char* ws, int slot) {
    return (v2u){__hip_atomic_load((gu32*)(ws + OFF_HS + HS_CH + slot * 4), RLX_AGENT), __hip_atomic_load((gu32*)(ws + OFF_HS + HS_EC + slot * 4), RLX_AGENT)};
}
__device__ __forceinline__ unsigned hs_challenge(unsigned char* ws, int slot, v2u old) {
    unsigned c = old.x + 1u; if (c == old.y) ++c;
    c = __builtin_amdgcn_readfirstlane(c);
    __hip_atomic_store((gu32*)(ws + OFF_HS + HS_CH + slot * 4), c, RLX_AGENT);
    return c;
}
__device__ __forceinline__ unsigned hs_begin(unsigned char* ws, int slot) { return hs_challenge(ws, slot, hs_peek(ws, slot)); }
template <bool INIT>
__device__ __forceinline__ bool hs_wait(unsigned char* ws, int slot, unsigned c, int seg = -1) {
    unsigned last = 0; bool first = true;
    for (unsigned spins = 0; spins < (1u << 21); ++spins) {
        const unsigned ev = __hip_atomic_load((gu32*)(ws + OFF_HS + HS_EC + slot * 4), RLX_AGENT);
        if (INIT) {
            const unsigned n = __builtin_amdgcn_readfirstlane(__hip_atomic_load((gu32*)(ws + OFF_HS + HS_B), RLX_AGENT));
            if (first || n != last) {
                if (seg >= 0) __hip_atomic_store((gu32*)(ws + OFF_HS + HS_X + seg * 64), n, RLX_AGENT);
                __hip_atomic_store((gu32*)(ws + OFF_HS + HS_R + slot * 4), n, RLX_AGENT); last = n; first = false;
            }
        }
        if ((unsigned)__builtin_amdgcn_readfirstlane(ev) == c) {
            __hip_atomic_store((gu32*)(ws + OFF_CTRL + C_PASS + slot * 4), 1u, RLX_AGENT);
            __builtin_amdgcn_fence(__ATOMIC_ACQUIRE, "wavefront");
            return true;
        }
        __builtin_amdgcn_s_sleep(INIT ? 1 : 2);
    }
    return false;
}
__device__ __forceinline__ bool slot_inits(int slot) { const int b = slot >> 2, x = b & 7, r = b >> 3; return r >= 16 && x >= 4; }
__device__ void hs_leader(unsigned char* ws, int myslot, int lane) {
    const rsrc_t rw = mk_rsrc(ws, WS_BYTES);
    v4u o[4];
#pragma unroll
    for (int i = 2; i < 4; ++i) o[i] = ld_sc1(rw, OFF_HS + HS_R + i * 1024 + lane * 16);
    const unsigned ox = __hip_atomic_load((gu32*)(ws + OFF_HS + HS_X + lane * 64), RLX_AGENT);
    __builtin_amdgcn_sched_barrier(0);
    unsigned n = (unsigned)__builtin_amdgcn_s_memrealtime() * 2654435761u;
    __hip_atomic_store((gu32*)(ws + OFF_HS + HS_B), n, RLX_AGENT);
    __builtin_amdgcn_sched_barrier(0);
    for (bool moved = false;; moved = true) {
        bool hit = ox == n;
#pragma unroll
        for (int i = 2; i < 4; ++i) hit = hit || o[i].x == n || o[i].y == n || o[i].z == n || o[i].w == n;
        if (!__any(hit)) { if (moved) __hip_atomic_store((gu32*)(ws + OFF_HS + HS_B), n, RLX_AGENT); break; }
        ++n;
    }
    bool ok = false;
    for (unsigned spins = 0; spins < (1u << 21) && !ok; ++spins) {
        asm volatile("" ::: "memory");
        bool all = true;
#pragma unroll
        for (int i = 2; i < 4; ++i) { const v4u v = ld_sc1(rw, OFF_HS + HS_R + i * 1024 + lane * 16); all = all && (!slot_inits(i * 256 + lane * 4) || (v.x == n && v.y == n && v.z == n && v.w == n)); }
        ok = __all(all);
    }
    if (!ok) return;
#pragma unroll
    for (int i = 0; i < 4; ++i) o[i] = ld_sc1(rw, OFF_HS + HS_CH + i * 1024 + lane * 16);
#pragma unroll
    for (int i = 3; i >= 0; --i) st_sc1(rw, OFF_HS + HS_EC + i * 1024 + lane * 16, o[i]);
    __hip_atomic_store((gu32*)(ws + OFF_CTRL + C_PASS + myslot * 4), 1u, RLX_AGENT);
    bool have_sel = false;
    for (unsigned spins = 0; spins < (1u << 21); ++spins) {
        asm volatile("" ::: "memory");
        bool all = true;
        const unsigned pr = __hip_atomic_load((gu32*)(ws + OFF_CTRL + C_PRB + (lane & 15) * 4), RLX_AGENT);
#pragma unroll
        for (int i = 0; i < 4; ++i) {
            const v4u c = ld_sc1(rw, OFF_HS + HS_CH + i * 1024 + lane * 16), ps = ld_sc1(rw, OFF_CTRL + C_PASS + i * 1024 + lane * 16);
            if (c.x != o[i].x || c.y != o[i].y || c.z != o[i].z || c.w != o[i].w) { st_sc1(rw, OFF_HS + HS_EC + i * 1024 + lane * 16, c); o[i] = c; }
            all = all && min(min(ps.x, ps.y), min(ps.z, ps.w)) != 0u;
        }
        if (!have_sel && __all(pr != 0u)) {
            unsigned b0 = pr >> 16, b1 = pr & 0xffffu;
#pragma unroll
            for (int m = 8; m >= 1; m >>= 1) { b0 = min(b0, (unsigned)__shfl_xor((int)b0, m)); b1 = min(b1, (unsigned)__shfl_xor((int)b1, m)); }
            __hip_atomic_store((gu32*)(ws + OFF_CTRL + C_SEL), b1 < b0 ? 2u : 1u, RLX_AGENT);
            have_sel = true;
        }
        if (have_sel && __all(all)) break;
        __builtin_amdgcn_s_sleep(2);
    }
}
__device__ __forceinline__ unsigned probe_pages(unsigned char* ws, int id, int lane) {
    unsigned char* pb = ws + MBASE + 131072 + (id & 7) * 32768 + 20480 + (id >> 3) * 2048;
    if (lane < 8) __hip_atomic_store((gu32*)(pb + (lane & 1) * 8192 + (lane >> 1) * 128), 0u, RLX_AGENT);
    DRAIN();
    unsigned d0 = 0xffffu, d1 = 0xffffu;
#pragma unroll 1
    for (int i = 0; i < 8; ++i) {
        const unsigned long long t0 = __builtin_amdgcn_s_memtime();
        asm volatile("s_waitcnt lgkmcnt(0)" ::: "memory");
        const unsigned v = __hip_atomic_load((gu32*)(pb + (i & 1) * 8192 + (i >> 1) * 128), RLX_AGENT);
        asm volatile("s_waitcnt vmcnt(0)" :: "v"(v) : "memory");
        const unsigned long long t1 = __builtin_amdgcn_s_memtime();
        const unsigned d = (unsigned)(t1 - t0);
        if (i & 1) d1 = min(d1, d); else d0 = min(d0, d);
    }
    return d0 << 16 | d1;
}
__device__ __forceinline__ unsigned wait_sel(unsigned char* ws) {
    if (!wait_ge(ws, OFF_CTRL + C_SEL, 1)) return ~0u;
    return (__builtin_amdgcn_readfirstlane(__hip_atomic_load((gu32*)(ws + OFF_CTRL + C_SEL), RLX_AGENT)) - 1u) << 13;
}
__device__ __forceinline__ void poison_fast(unsigned char* ws, int i) {
    const rsrc_t rw = mk_rsrc(ws, WS_BYTES);
    const int tid = i * 256 + threadIdx.x; constexpr int nthr = 64 * 256, NF = 64 - FAST0;
    const v4u ones = {~0u, ~0u, ~0u, ~0u};
    for (int k = tid + (int)(CTRL_URGENT / 16); k < (int)(CTRL_BYTES / 16); k += nthr) st_sc1(rw, OFF_CTRL + k * 16, (v4u){0u, 0u, 0u, 0u});
    for (int k = tid; k < NF * 8192; k += nthr) { st_sc1(rw, OFF_KM + FAST0 * 131072 + k * 16, ones); st_sc1(rw, OFF_VM + FAST0 * 131072 + k * 16, ones); }
    for (int k = tid; k < NF * 16384; k += nthr) st_sc1(rw, OFF_OF + FAST0 * 262144 + k * 16, ones);
    for (int k = tid; k < NF * 36864; k += nthr) st_sc1(rw, OFF_WP + FAST0 * 589824 + k * 16, ones);
    DRAIN();
    __syncthreads();
    if (threadIdx.x == 0) __hip_atomic_store((gu32*)(ws + OFF_CTRL + C_XFL + i * 64 + 4), 1u, RLX_AGENT);
}
__device__ __forceinline__ bool wait_deferred(unsigned char* ws, int lane) {
    gu32* w = (gu32*)(ws + OFF_CTRL + C_XFL + lane * 64 + 4);
    for (unsigned spins = 0; !__all(__hip_atomic_load(w, RLX_AGENT) != 0u); ++spins) { __builtin_amdgcn_s_sleep(8); if (spins > (1u << 20)) { set_tmo(ws); return false; } }
    __builtin_amdgcn_fence(__ATOMIC_ACQUIRE, "wavefront");
    return true;
}
__device__ __forceinline__ void init_ws(unsigned char* ws, const unsigned short* mem0, int i) {
    const rsrc_t rw = mk_rsrc(ws, WS_BYTES);
    const int tid = i * 256 + threadIdx.x; constexpr int nthr = 64 * 256;
    const v4u ones = {~0u, ~0u, ~0u, ~0u}, zero = {0u, 0u, 0u, 0u};
    const int q = tid & 2047, ch = q < 1024 ? (q & 511) : ((q & 511) < 64 ? 512 + (q & 63) : -1);
    unsigned e[8];
    if (tid < 8192 && ch >= 0) {
        const int ks = ch >> 6, rho = ch & 15, g = (ch >> 4) & 3, tok = 2 * (rho >> 2) + (rho & 1), eta = (rho >> 1) & 1;
#pragma unroll
        for (int j = 0; j < 8; ++j) e[j] = mem0[tok * 576 + colset(ks, eta, g, j)];
    }
    __builtin_amdgcn_sched_barrier(0);
    for (int k = tid; k < (int)(CTRL_URGENT / 16); k += nthr) st_sc1(rw, OFF_CTRL + k * 16, zero);
    for (int k = tid; k < 36 * 1024; k += nthr) st_sc1(rw, k * 16, ones);
    st_sc1(rw, VRBASE + tid * 16, ones); st_sc1(rw, VRBASE + 262144 + tid * 16, ones);
    if (tid >= 8192 && ch >= 0) st_sc1(rw, MBASE + tid * 16, ones);
    __builtin_amdgcn_sched_barrier(0);
    if (tid < 8192 && ch >= 0) st_sc1(rw, MBASE + tid * 16, (v4u){min(e[0] | (e[1] << 16), ~1u), min(e[2] | (e[3] << 16), ~1u), min(e[4] | (e[5] << 16), ~1u), min(e[6] | (e[7] << 16), ~1u)});
}

__device__ __forceinline__ void head_asm(rsrc_t rq, rsrc_t rk, rsrc_t ro, rsrc_t rw, int h, int w, int slot) {
    asm volatile(
    "s_lshl_b32 s98, %[slot], 2\n\t"
    "s_add_u32 s98, s98, 3149824\n\t"
    "buffer_load_dword v72, off, %[rw], s98 sc1\n\t"
    "s_add_u32 s98, s98, 4096\n\t"
    "buffer_load_dword v73, off, %[rw], s98 sc1\n\t"
    "s_sub_u32 s98, s98, 4096\n\t"
    "v_mbcnt_lo_u32_b32 v76, -1, 0\n\t"
    "v_mbcnt_hi_u32_b32 v76, -1, v76\n\t"
    "s_nop 1\n\t"
    "s_lshl_b32 s60, %[w], 15\n\t"
    "s_lshl_b32 s74, %[w], 11\n\t"
    "v_lshrrev_b32 v12, 2, v76\n\t"
    "v_lshlrev_b32 v77, 6, v12\n\t"
    "v_bfe_u32 v12, v76, 4, 1\n\t"
    "v_lshl_add_u32 v77, v12, 5, v77\n\t"
    "v_and_b32 v12, 3, v76\n\t"
    "v_lshl_add_u32 v77, v12, 3, v77\n\t"
    "v_add_u32 v77, s60, v77\n\t"
    "v_xor_b32 v78, 32, v77\n\t"
    "v_bfe_u32 v20, v76, 4, 1\n\t"
    "v_lshrrev_b32 v21, 5, v76\n\t"
    "v_bfe_u32 v22, v76, 2, 1\n\t"
    "v_bfe_u32 v23, v76, 3, 1\n\t"
    "v_bfe_u32 v12, v76, 1, 1\n\t"
    "v_xor_b32 v12, v12, v20\n\t"
    "v_lshlrev_b32 v19, 5, v12\n\t"
    "v_and_b32 v12, 1, v76\n\t"
    "v_lshl_add_u32 v19, v12, 4, v19\n\t"
    "v_lshlrev_b32 v12, 6, v20\n\t"
    "v_lshl_add_u32 v12, v22, 4, v12\n\t"
    "v_lshl_add_u32 v12, v21, 1, v12\n\t"
    "v_add_u32 v12, v12, v23\n\t"
    "v_lshl_add_u32 v16, v12, 13, v19\n\t"
    "v_mul_u32_u24 v12, 144, v21\n\t"
    "v_lshl_add_u32 v12, v23, 3, v12\n\t"
    "v_lshl_add_u32 v12, v20, 2, v12\n\t"
    "v_add_u32 v12, v12, v22\n\t"
    "v_lshl_add_u32 v17, v12, 13, v19\n\t"
    "v_lshlrev_b32 v12, 4, v23\n\t"
    "v_lshl_add_u32 v12, v20, 2, v12\n\t"
    "v_lshl_add_u32 v12, v21, 1, v12\n\t"
    "v_add_u32 v12, v12, v22\n\t"
    "v_mul_u32_u24 v18, 1152, v12\n\t"
    "v_and_b32 v12, 3, v76\n\t"
    "v_lshl_add_u32 v18, v12, 4, v18\n\t"
    "v_lshlrev_b32 v79, 3, v12\n\t"
    "v_lshl_add_u32 v79, v23, 5, v79\n\t"
    "v_lshl_add_u32 v79, v22, 6, v79\n\t"
    "v_lshrrev_b32 v12, 4, v76\n\t"
    "v_lshl_add_u32 v79, v12, 7, v79\n\t"
    "v_add_u32 v79, s60, v79\n\t"
    "s_lshl_b32 s86, %[h], 8\n\t"
    "s_lshl_b32 s61, %[w], 6\n\t"
    "s_add_u32 s86, s86, s61\n\t"
    "s_mul_i32 s87, %[h], 147456\n\t"
    "s_mul_i32 s61, %[w], 288\n\t"
    "s_add_u32 s87, s87, s61\n\t"
    "s_xor_b32 s61, %[w], 0\n\t"
    "s_mul_i32 s61, s61, 36864\n\t"
    "s_add_u32 s80, s61, s87\n\t"
    "s_xor_b32 s61, %[w], 1\n\t"
    "s_mul_i32 s61, s61, 36864\n\t"
    "s_add_u32 s81, s61, s87\n\t"
    "s_xor_b32 s61, %[w], 2\n\t"
    "s_mul_i32 s61, s61, 36864\n\t"
    "s_add_u32 s82, s61, s87\n\t"
    "s_xor_b32 s61, %[w], 3\n\t"
    "s_mul_i32 s61, s61, 36864\n\t"
    "s_add_u32 s83, s61, s87\n\t"
    "s_xor_b32 s61, %[w], 0\n\t"
    "s_mul_i32 s61, s61, 1179648\n\t"
    "s_add_u32 s64, s61, s86\n\t"
    "s_xor_b32 s61, %[w], 1\n\t"
    "s_mul_i32 s61, s61, 1179648\n\t"
    "s_add_u32 s65, s61, s86\n\t"
    "s_xor_b32 s61, %[w], 2\n\t"
    "s_mul_i32 s61, s61, 1179648\n\t"
    "s_add_u32 s66, s61, s86\n\t"
    "s_xor_b32 s61, %[w], 3\n\t"
    "s_mul_i32 s61, s61, 1179648\n\t"
    "s_add_u32 s67, s61, s86\n\t"
    "s_add_u32 s61, s64, 0\n\t"
    "s_add_u32 m0, s60, 0\n\t"
    "s_nop 0\n\t"
    "buffer_load_dwordx4 v16, %[rk], s61 offen lds\n\t"
    "s_add_u32 s61, s64, 262144\n\t"
    "s_add_u32 m0, s60, 1024\n\t"
    "s_nop 0\n\t"
    "buffer_load_dwordx4 v16, %[rk], s61 offen lds\n\t"
    "s_add_u32 s61, s64, 32768\n\t"
    "s_add_u32 m0, s60, 2048\n\t"
    "s_nop 0\n\t"
    "buffer_load_dwordx4 v16, %[rk], s61 offen lds\n\t"
    "s_add_u32 s61, s64, 294912\n\t"
    "s_add_u32 m0, s60, 3072\n\t"
    "s_nop 0\n\t"
    "buffer_load_dwordx4 v16, %[rk], s61 offen lds\n\t"
    "s_add_u32 s61, s64, 65536\n\t"
    "s_add_u32 m0, s60, 4096\n\t"
    "s_nop 0\n\t"
    "buffer_load_dwordx4 v16, %[rk], s61 offen lds\n\t"
    "s_add_u32 s61, s64, 327680\n\t"
    "s_add_u32 m0, s60, 5120\n\t"
    "s_nop 0\n\t"
    "buffer_load_dwordx4 v16, %[rk], s61 offen lds\n\t"
    "s_add_u32 s61, s64, 98304\n\t"
    "s_add_u32 m0, s60, 6144\n\t"
    "s_nop 0\n\t"
    "buffer_load_dwordx4 v16, %[rk], s61 offen lds\n\t"
    "s_add_u32 s61, s64, 360448\n\t"
    "s_add_u32 m0, s60, 7168\n\t"
    "s_nop 0\n\t"
    "buffer_load_dwordx4 v16, %[rk], s61 offen lds\n\t"
    "s_add_u32 s61, s65, 0\n\t"
    "s_add_u32 m0, s60, 8192\n\t"
    "s_nop 0\n\t"
    "buffer_load_dwordx4 v16, %[rk], s61 offen lds\n\t"
    "s_add_u32 s61, s65, 262144\n\t"
    "s_add_u32 m0, s60, 9216\n\t"
    "s_nop 0\n\t"
    "buffer_load_dwordx4 v16, %[rk], s61 offen lds\n\t"
    "s_add_u32 s61, s65, 32768\n\t"
    "s_add_u32 m0, s60, 10240\n\t"
    "s_nop 0\n\t"
    "buffer_load_dwordx4 v16, %[rk], s61 offen lds\n\t"
    "s_add_u32 s61, s65, 294912\n\t"
    "s_add_u32 m0, s60, 11264\n\t"
    "s_nop 0\n\t"
    "buffer_load_dwordx4 v16, %[rk], s61 offen lds\n\t"
    "s_add_u32 s61, s65, 65536\n\t"
    "s_add_u32 m0, s60, 12288\n\t"
    "s_nop 0\n\t"
    "buffer_load_dwordx4 v16, %[rk], s61 offen lds\n\t"
    "s_add_u32 s61, s65, 327680\n\t"
    "s_add_u32 m0, s60, 13312\n\t"
    "s_nop 0\n\t"
    "buffer_load_dwordx4 v16, %[rk], s61 offen lds\n\t"
    "s_add_u32 s61, s65, 98304\n\t"
    "s_add_u32 m0, s60, 14336\n\t"
    "s_nop 0\n\t"
    "buffer_load_dwordx4 v16, %[rk], s61 offen lds\n\t"
    "s_add_u32 s61, s65, 360448\n\t"
    "s_add_u32 m0, s60, 15360\n\t"
    "s_nop 0\n\t"
    "buffer_load_dwordx4 v16, %[rk], s61 offen lds\n\t"
    "s_add_u32 s61, s66, 0\n\t"
    "s_add_u32 m0, s60, 16384\n\t"
    "s_nop 0\n\t"
    "buffer_load_dwordx4 v16, %[rk], s61 offen lds\n\t"
    "s_add_u32 s61, s66, 262144\n\t"
    "s_add_u32 m0, s60, 17408\n\t"
    "s_nop 0\n\t"
    "buffer_load_dwordx4 v16, %[rk], s61 offen lds\n\t"
    "s_add_u32 s61, s66, 32768\n\t"
    "s_add_u32 m0, s60, 18432\n\t"
    "s_nop 0\n\t"
    "buffer_load_dwordx4 v16, %[rk], s61 offen lds\n\t"
    "s_add_u32 s61, s66, 294912\n\t"
    "s_add_u32 m0, s60, 19456\n\t"
    "s_nop 0\n\t"
    "buffer_load_dwordx4 v16, %[rk], s61 offen lds\n\t"
    "s_add_u32 s61, s66, 65536\n\t"
    "s_add_u32 m0, s60, 20480\n\t"
    "s_nop 0\n\t"
    "buffer_load_dwordx4 v16, %[rk], s61 offen lds\n\t"
    "s_add_u32 s61, s66, 327680\n\t"
    "s_add_u32 m0, s60, 21504\n\t"
    "s_nop 0\n\t"
    "buffer_load_dwordx4 v16, %[rk], s61 offen lds\n\t"
    "s_add_u32 s61, s66, 98304\n\t"
    "s_add_u32 m0, s60, 22528\n\t"
    "s_nop 0\n\t"
    "buffer_load_dwordx4 v16, %[rk], s61 offen lds\n\t"
    "s_add_u32 s61, s66, 360448\n\t"
    "s_add_u32 m0, s60, 23552\n\t"
    "s_nop 0\n\t"
    "buffer_load_dwordx4 v16, %[rk], s61 offen lds\n\t"
    "s_add_u32 s61, s67, 0\n\t"
    "s_add_u32 m0, s60, 24576\n\t"
    "s_nop 0\n\t"
    "buffer_load_dwordx4 v16, %[rk], s61 offen lds\n\t"
    "s_add_u32 s61, s67, 262144\n\t"
    "s_add_u32 m0, s60, 25600\n\t"
    "s_nop 0\n\t"
    "buffer_load_dwordx4 v16, %[rk], s61 offen lds\n\t"
    "s_add_u32 s61, s67, 32768\n\t"
    "s_add_u32 m0, s60, 26624\n\t"
    "s_nop 0\n\t"
    "buffer_load_dwordx4 v16, %[rk], s61 offen lds\n\t"
    "s_add_u32 s61, s67, 294912\n\t"
    "s_add_u32 m0, s60, 27648\n\t"
    "s_nop 0\n\t"
    "buffer_load_dwordx4 v16, %[rk], s61 offen lds\n\t"
    "s_add_u32 s61, s67, 65536\n\t"
    "s_add_u32 m0, s60, 28672\n\t"
    "s_nop 0\n\t"
    "buffer_load_dwordx4 v16, %[rk], s61 offen lds\n\t"
    "s_add_u32 s61, s67, 327680\n\t"
    "s_add_u32 m0, s60, 29696\n\t"
    "s_nop 0\n\t"
    "buffer_load_dwordx4 v16, %[rk], s61 offen lds\n\t"
    "s_add_u32 s61, s67, 98304\n\t"
    "s_add_u32 m0, s60, 30720\n\t"
    "s_nop 0\n\t"
    "buffer_load_dwordx4 v16, %[rk], s61 offen lds\n\t"
    "s_add_u32 s61, s67, 360448\n\t"
    "s_add_u32 m0, s60, 31744\n\t"
    "s_nop 0\n\t"
    "buffer_load_dwordx4 v16, %[rk], s61 offen lds\n\t"
    "v_lshlrev_b32 v12, 4, v76\n\t"
    "v_add_u32 v0, 131072, v12\n\t"
    "v_add_u32 v2, s74, v0\n\t"
    "s_lshl_b32 s61, %[w], 10\n\t"
    "s_add_u32 s61, s61, 12288\n\t"
    "v_add_u32 v5, s61, v12\n\t"
    "v_xor_b32 v9, 2048, v2\n\t"
    "v_xor_b32 v10, 4096, v2\n\t"
    "v_xor_b32 v11, 6144, v2\n\t"
    "v_lshrrev_b32 v24, 4, v76\n\t"
    "v_and_b32 v25, 15, v76\n\t"
    "v_bfe_u32 v12, v76, 1, 1\n\t"
    "v_mul_u32_u24 v12, 43690, v12\n\t"
    "v_add_u32 v1, 17476, v12\n\t"
    "v_mul_u32_u24 v12, 1088, v24\n\t"
    "v_lshl_add_u32 v12, v25, 3, v12\n\t"
    "s_lshl_b32 s61, %[w], 8\n\t"
    "v_add_u32 v3, s61, v12\n\t"
    "v_and_b32 v12, 3, v76\n\t"
    "v_mul_u32_u24 v12, 1088, v12\n\t"
    "v_lshrrev_b32 v13, 2, v76\n\t"
    "v_lshl_add_u32 v4, v13, 3, v12\n\t"
    "v_lshrrev_b32 v12, 5, v76\n\t"
    "v_lshlrev_b32 v26, 6, v12\n\t"
    "v_bfe_u32 v12, v76, 4, 1\n\t"
    "v_lshl_add_u32 v26, v12, 4, v26\n\t"
    "v_and_b32 v13, 1, v76\n\t"
    "v_lshl_add_u32 v6, v13, 5, v26\n\t"
    "v_bfe_u32 v12, v76, 1, 3\n\t"
    "v_lshl_add_u32 v6, v12, 14, v6\n\t"
    "v_lshl_add_u32 v7, v13, 12, v26\n\t"
    "v_bfe_u32 v12, v76, 3, 1\n\t"
    "v_lshl_add_u32 v7, v12, 5, v7\n\t"
    "v_bfe_u32 v12, v76, 1, 2\n\t"
    "v_lshl_add_u32 v7, v12, 2, v7\n\t"
    "s_mov_b32 s61, 0xfdfe0606\n\t"
    "v_mul_lo_u32 v8, s61, v13\n\t"
    "v_add_u32 v8, 84148480, v8\n\t"
    "s_waitcnt vmcnt(33)\n\t"
    "v_add_u32 v74, 1, v72\n\t"
    "v_add_u32 v75, 2, v72\n\t"
    "s_waitcnt vmcnt(32)\n\t"
    "v_cmp_eq_u32 vcc, v74, v73\n\t"
    "s_nop 1\n\t"
    "v_cndmask_b32 v74, v74, v75, vcc\n\t"
    "s_nop 0\n\t"
    "v_readfirstlane_b32 s99, v74\n\t"
    "buffer_store_dword v74, off, %[rw], s98 sc1\n\t"
    "s_add_u32 s98, s98, 4096\n\t"
    "s_waitcnt vmcnt(29)\n\t"
    "ds_read_b64_tr_b16 a[0:1], v77 offset:0\n\t"
    "ds_read_b64_tr_b16 a[2:3], v77 offset:1024\n\t"
    "ds_read_b64_tr_b16 a[4:5], v77 offset:2048\n\t"
    "ds_read_b64_tr_b16 a[6:7], v77 offset:3072\n\t"
    "ds_read_b64_tr_b16 a[8:9], v78 offset:0\n\t"
    "ds_read_b64_tr_b16 a[10:11], v78 offset:1024\n\t"
    "ds_read_b64_tr_b16 a[12:13], v78 offset:2048\n\t"
    "ds_read_b64_tr_b16 a[14:15], v78 offset:3072\n\t"
    "s_waitcnt vmcnt(25)\n\t"
    "ds_read_b64_tr_b16 a[16:17], v77 offset:4096\n\t"
    "ds_read_b64_tr_b16 a[18:19], v77 offset:5120\n\t"
    "ds_read_b64_tr_b16 a[20:21], v77 offset:6144\n\t"
    "ds_read_b64_tr_b16 a[22:23], v77 offset:7168\n\t"
    "ds_read_b64_tr_b16 a[24:25], v78 offset:4096\n\t"
    "ds_read_b64_tr_b16 a[26:27], v78 offset:5120\n\t"
    "ds_read_b64_tr_b16 a[28:29], v78 offset:6144\n\t"
    "s_waitcnt lgkmcnt(14)\n\t"
    "ds_read_b64_tr_b16 a[30:31], v78 offset:7168\n\t"
    "s_waitcnt lgkmcnt(8)\n\t"
    "s_add_u32 s61, s86, 1048576\n\t"
    "s_add_u32 m0, s60, 0\n\t"
    "s_nop 0\n\t"
    "buffer_load_dwordx4 v17, %[rk], s61 offen lds\n\t"
    "s_add_u32 s61, s86, 1064960\n\t"
    "s_add_u32 m0, s60, 1024\n\t"
    "s_nop 0\n\t"
    "buffer_load_dwordx4 v17, %[rk], s61 offen lds\n\t"
    "s_add_u32 s61, s86, 3407872\n\t"
    "s_add_u32 m0, s60, 2048\n\t"
    "s_nop 0\n\t"
    "buffer_load_dwordx4 v17, %[rk], s61 offen lds\n\t"
    "s_add_u32 s61, s86, 3424256\n\t"
    "s_add_u32 m0, s60, 3072\n\t"
    "s_nop 0\n\t"
    "buffer_load_dwordx4 v17, %[rk], s61 offen lds\n\t"
    "s_waitcnt vmcnt(25)\n\t"
    "ds_read_b64_tr_b16 a[32:33], v77 offset:8192\n\t"
    "ds_read_b64_tr_b16 a[34:35], v77 offset:9216\n\t"
    "ds_read_b64_tr_b16 a[36:37], v77 offset:10240\n\t"
    "ds_read_b64_tr_b16 a[38:39], v77 offset:11264\n\t"
    "ds_read_b64_tr_b16 a[40:41], v78 offset:8192\n\t"
    "ds_read_b64_tr_b16 a[42:43], v78 offset:9216\n\t"
    "ds_read_b64_tr_b16 a[44:45], v78 offset:10240\n\t"
    "s_waitcnt lgkmcnt(14)\n\t"
    "ds_read_b64_tr_b16 a[46:47], v78 offset:11264\n\t"
    "s_waitcnt lgkmcnt(8)\n\t"
    "s_add_u32 s61, s64, 0\n\t"
    "s_add_u32 m0, s60, 4096\n\t"
    "s_nop 0\n\t"
    "buffer_load_dwordx4 v16, %[rq], s61 offen lds\n\t"
    "s_add_u32 s61, s64, 262144\n\t"
    "s_add_u32 m0, s60, 5120\n\t"
    "s_nop 0\n\t"
    "buffer_load_dwordx4 v16, %[rq], s61 offen lds\n\t"
    "s_add_u32 s61, s64, 32768\n\t"
    "s_add_u32 m0, s60, 6144\n\t"
    "s_nop 0\n\t"
    "buffer_load_dwordx4 v16, %[rq], s61 offen lds\n\t"
    "s_add_u32 s61, s64, 294912\n\t"
    "s_add_u32 m0, s60, 7168\n\t"
    "s_nop 0\n\t"
    "buffer_load_dwordx4 v16, %[rq], s61 offen lds\n\t"
    "s_waitcnt vmcnt(25)\n\t"
    "ds_read_b64_tr_b16 a[48:49], v77 offset:12288\n\t"
    "ds_read_b64_tr_b16 a[50:51], v77 offset:13312\n\t"
    "ds_read_b64_tr_b16 a[52:53], v77 offset:14336\n\t"
    "ds_read_b64_tr_b16 a[54:55], v77 offset:15360\n\t"
    "ds_read_b64_tr_b16 a[56:57], v78 offset:12288\n\t"
    "ds_read_b64_tr_b16 a[58:59], v78 offset:13312\n\t"
    "ds_read_b64_tr_b16 a[60:61], v78 offset:14336\n\t"
    "s_waitcnt lgkmcnt(14)\n\t"
    "ds_read_b64_tr_b16 a[62:63], v78 offset:15360\n\t"
    "s_waitcnt lgkmcnt(8)\n\t"
    "s_add_u32 s61, s64, 65536\n\t"
    "s_add_u32 m0, s60, 8192\n\t"
    "s_nop 0\n\t"
    "buffer_load_dwordx4 v16, %[rq], s61 offen lds\n\t"
    "s_add_u32 s61, s64, 327680\n\t"
    "s_add_u32 m0, s60, 9216\n\t"
    "s_nop 0\n\t"
    "buffer_load_dwordx4 v16, %[rq], s61 offen lds\n\t"
    "s_add_u32 s61, s64, 98304\n\t"
    "s_add_u32 m0, s60, 10240\n\t"
    "s_nop 0\n\t"
    "buffer_load_dwordx4 v16, %[rq], s61 offen lds\n\t"
    "s_add_u32 s61, s64, 360448\n\t"
    "s_add_u32 m0, s60, 11264\n\t"
    "s_nop 0\n\t"
    "buffer_load_dwordx4 v16, %[rq], s61 offen lds\n\t"
    "s_waitcnt vmcnt(25)\n\t"
    "ds_read_b64_tr_b16 a[64:65], v77 offset:16384\n\t"
    "ds_read_b64_tr_b16 a[66:67], v77 offset:17408\n\t"
    "ds_read_b64_tr_b16 a[68:69], v77 offset:18432\n\t"
    "ds_read_b64_tr_b16 a[70:71], v77 offset:19456\n\t"
    "ds_read_b64_tr_b16 a[72:73], v78 offset:16384\n\t"
    "ds_read_b64_tr_b16 a[74:75], v78 offset:17408\n\t"
    "ds_read_b64_tr_b16 a[76:77], v78 offset:18432\n\t"
    "s_waitcnt lgkmcnt(14)\n\t"
    "ds_read_b64_tr_b16 a[78:79], v78 offset:19456\n\t"
    "s_waitcnt lgkmcnt(8)\n\t"
    "s_add_u32 s61, s65, 0\n\t"
    "s_add_u32 m0, s60, 12288\n\t"
    "s_nop 0\n\t"
    "buffer_load_dwordx4 v16, %[rq], s61 offen lds\n\t"
    "s_add_u32 s61, s65, 262144\n\t"
    "s_add_u32 m0, s60, 13312\n\t"
    "s_nop 0\n\t"
    "buffer_load_dwordx4 v16, %[rq], s61 offen lds\n\t"
    "s_add_u32 s61, s65, 32768\n\t"
    "s_add_u32 m0, s60, 14336\n\t"
    "s_nop 0\n\t"
    "buffer_load_dwordx4 v16, %[rq], s61 offen lds\n\t"
    "s_add_u32 s61, s65, 294912\n\t"
    "s_add_u32 m0, s60, 15360\n\t"
    "s_nop 0\n\t"
    "buffer_load_dwordx4 v16, %[rq], s61 offen lds\n\t"
    "s_waitcnt vmcnt(25)\n\t"
    "ds_read_b64_tr_b16 a[80:81], v77 offset:20480\n\t"
    "ds_read_b64_tr_b16 a[82:83], v77 offset:21504\n\t"
    "ds_read_b64_tr_b16 a[84:85], v77 offset:22528\n\t"
    "ds_read_b64_tr_b16 a[86:87], v77 offset:23552\n\t"
    "ds_read_b64_tr_b16 a[88:89], v78 offset:20480\n\t"
    "ds_read_b64_tr_b16 a[90:91], v78 offset:21504\n\t"
    "ds_read_b64_tr_b16 a[92:93], v78 offset:22528\n\t"
    "s_waitcnt lgkmcnt(14)\n\t"
    "ds_read_b64_tr_b16 a[94:95], v78 offset:23552\n\t"
    "s_waitcnt lgkmcnt(8)\n\t"
    "s_add_u32 s61, s65, 65536\n\t"
    "s_add_u32 m0, s60, 16384\n\t"
    "s_nop 0\n\t"
    "buffer_load_dwordx4 v16, %[rq], s61 offen lds\n\t"
    "s_add_u32 s61, s65, 327680\n\t"
    "s_add_u32 m0, s60, 17408\n\t"
    "s_nop 0\n\t"
    "buffer_load_dwordx4 v16, %[rq], s61 offen lds\n\t"
    "s_add_u32 s61, s65, 98304\n\t"
    "s_add_u32 m0, s60, 18432\n\t"
    "s_nop 0\n\t"
    "buffer_load_dwordx4 v16, %[rq], s61 offen lds\n\t"
    "s_add_u32 s61, s65, 360448\n\t"
    "s_add_u32 m0, s60, 19456\n\t"
    "s_nop 0\n\t"
    "buffer_load_dwordx4 v16, %[rq], s61 offen lds\n\t"
    "s_waitcnt vmcnt(25)\n\t"
    "ds_read_b64_tr_b16 a[96:97], v77 offset:24576\n\t"
    "ds_read_b64_tr_b16 a[98:99], v77 offset:25600\n\t"
    "ds_read_b64_tr_b16 a[100:101], v77 offset:26624\n\t"
    "ds_read_b64_tr_b16 a[102:103], v77 offset:27648\n\t"
    "ds_read_b64_tr_b16 a[104:105], v78 offset:24576\n\t"
    "ds_read_b64_tr_b16 a[106:107], v78 offset:25600\n\t"
    "ds_read_b64_tr_b16 a[108:109], v78 offset:26624\n\t"
    "s_waitcnt lgkmcnt(14)\n\t"
    "ds_read_b64_tr_b16 a[110:111], v78 offset:27648\n\t"
    "s_waitcnt lgkmcnt(8)\n\t"
    "s_add_u32 s61, s66, 0\n\t"
    "s_add_u32 m0, s60, 20480\n\t"
    "s_nop 0\n\t"
    "buffer_load_dwordx4 v16, %[rq], s61 offen lds\n\t"
    "s_add_u32 s61, s66, 262144\n\t"
    "s_add_u32 m0, s60, 21504\n\t"
    "s_nop 0\n\t"
    "buffer_load_dwordx4 v16, %[rq], s61 offen lds\n\t"
    "s_add_u32 s61, s66, 32768\n\t"
    "s_add_u32 m0, s60, 22528\n\t"
    "s_nop 0\n\t"
    "buffer_load_dwordx4 v16, %[rq], s61 offen lds\n\t"
    "s_add_u32 s61, s66, 294912\n\t"
    "s_add_u32 m0, s60, 23552\n\t"
    "s_nop 0\n\t"
    "buffer_load_dwordx4 v16, %[rq], s61 offen lds\n\t"
    "s_waitcnt vmcnt(25)\n\t"
    "ds_read_b64_tr_b16 a[112:113], v77 offset:28672\n\t"
    "ds_read_b64_tr_b16 a[114:115], v77 offset:29696\n\t"
    "ds_read_b64_tr_b16 a[116:117], v77 offset:30720\n\t"
    "ds_read_b64_tr_b16 a[118:119], v77 offset:31744\n\t"
    "ds_read_b64_tr_b16 a[120:121], v78 offset:28672\n\t"
    "ds_read_b64_tr_b16 a[122:123], v78 offset:29696\n\t"
    "ds_read_b64_tr_b16 a[124:125], v78 offset:30720\n\t"
    "s_waitcnt lgkmcnt(14)\n\t"
    "ds_read_b64_tr_b16 a[126:127], v78 offset:31744\n\t"
    "s_waitcnt lgkmcnt(8)\n\t"
    "s_add_u32 s61, s66, 65536\n\t"
    "s_add_u32 m0, s60, 24576\n\t"
    "s_nop 0\n\t"
    "buffer_load_dwordx4 v16, %[rq], s61 offen lds\n\t"
    "s_add_u32 s61, s66, 327680\n\t"
    "s_add_u32 m0, s60, 25600\n\t"
    "s_nop 0\n\t"
    "buffer_load_dwordx4 v16, %[rq], s61 offen lds\n\t"
    "s_add_u32 s61, s66, 98304\n\t"
    "s_add_u32 m0, s60, 26624\n\t"
    "s_nop 0\n\t"
    "buffer_load_dwordx4 v16, %[rq], s61 offen lds\n\t"
    "s_add_u32 s61, s66, 360448\n\t"
    "s_add_u32 m0, s60, 27648\n\t"
    "s_nop 0\n\t"
    "buffer_load_dwordx4 v16, %[rq], s61 offen lds\n\t"
    "s_waitcnt vmcnt(24)\n\t"
    "ds_read_b64_tr_b16 a[128:129], v77 offset:0\n\t"
    "ds_read_b64_tr_b16 a[130:131], v77 offset:1024\n\t"
    "ds_read_b64_tr_b16 a[132:133], v77 offset:2048\n\t"
    "ds_read_b64_tr_b16 a[134:135], v77 offset:3072\n\t"
    "ds_read_b64_tr_b16 a[136:137], v78 offset:0\n\t"
    "ds_read_b64_tr_b16 a[138:139], v78 offset:1024\n\t"
    "ds_read_b64_tr_b16 a[140:141], v78 offset:2048\n\t"
    "s_waitcnt lgkmcnt(14)\n\t"
    "ds_read_b64_tr_b16 a[142:143], v78 offset:3072\n\t"
    "s_waitcnt lgkmcnt(8)\n\t"
    "s_add_u32 s61, s67, 0\n\t"
    "s_add_u32 m0, s60, 28672\n\t"
    "s_nop 0\n\t"
    "buffer_load_dwordx4 v16, %[rq], s61 offen lds\n\t"
    "s_add_u32 s61, s67, 262144\n\t"
    "s_add_u32 m0, s60, 29696\n\t"
    "s_nop 0\n\t"
    "buffer_load_dwordx4 v16, %[rq], s61 offen lds\n\t"
    "s_add_u32 s61, s67, 32768\n\t"
    "s_add_u32 m0, s60, 30720\n\t"
    "s_nop 0\n\t"
    "buffer_load_dwordx4 v16, %[rq], s61 offen lds\n\t"
    "s_add_u32 s61, s67, 294912\n\t"
    "s_add_u32 m0, s60, 31744\n\t"
    "s_nop 0\n\t"
    "buffer_load_dwordx4 v16, %[rq], s61 offen lds\n\t"
    "s_waitcnt vmcnt(24)\n\t"
    "ds_read_b64_tr_b16 a[144:145], v77 offset:4096\n\t"
    "ds_read_b64_tr_b16 a[146:147], v77 offset:5120\n\t"
    "ds_read_b64_tr_b16 a[148:149], v77 offset:6144\n\t"
    "ds_read_b64_tr_b16 a[150:151], v77 offset:7168\n\t"
    "ds_read_b64_tr_b16 a[152:153], v78 offset:4096\n\t"
    "ds_read_b64_tr_b16 a[154:155], v78 offset:5120\n\t"
    "ds_read_b64_tr_b16 a[156:157], v78 offset:6144\n\t"
    "s_waitcnt lgkmcnt(14)\n\t"
    "ds_read_b64_tr_b16 a[158:159], v78 offset:7168\n\t"
    "s_waitcnt lgkmcnt(8)\n\t"
    "s_add_u32 s61, s67, 65536\n\t"
    "s_add_u32 m0, s60, 0\n\t"
    "s_nop 0\n\t"
    "buffer_load_dwordx4 v16, %[rq], s61 offen lds\n\t"
    "s_add_u32 s61, s67, 327680\n\t"
    "s_add_u32 m0, s60, 1024\n\t"
    "s_nop 0\n\t"
    "buffer_load_dwordx4 v16, %[rq], s61 offen lds\n\t"
    "s_add_u32 s61, s67, 98304\n\t"
    "s_add_u32 m0, s60, 2048\n\t"
    "s_nop 0\n\t"
    "buffer_load_dwordx4 v16, %[rq], s61 offen lds\n\t"
    "s_add_u32 s61, s67, 360448\n\t"
    "s_add_u32 m0, s60, 3072\n\t"
    "s_nop 0\n\t"
    "buffer_load_dwordx4 v16, %[rq], s61 offen lds\n\t"
    "s_waitcnt vmcnt(24)\n\t"
    "ds_read_b64_tr_b16 a[160:161], v77 offset:8192\n\t"
    "ds_read_b64_tr_b16 a[162:163], v77 offset:9216\n\t"
    "ds_read_b64_tr_b16 a[164:165], v77 offset:10240\n\t"
    "ds_read_b64_tr_b16 a[166:167], v77 offset:11264\n\t"
    "ds_read_b64_tr_b16 a[168:169], v78 offset:8192\n\t"
    "ds_read_b64_tr_b16 a[170:171], v78 offset:9216\n\t"
    "ds_read_b64_tr_b16 a[172:173], v78 offset:10240\n\t"
    "s_waitcnt lgkmcnt(14)\n\t"
    "ds_read_b64_tr_b16 a[174:175], v78 offset:11264\n\t"
    "s_waitcnt lgkmcnt(8)\n\t"
    "s_add_u32 s61, s86, 1048576\n\t"
    "s_add_u32 m0, s60, 4096\n\t"
    "s_nop 0\n\t"
    "buffer_load_dwordx4 v17, %[rq], s61 offen lds\n\t"
    "s_add_u32 s61, s86, 1064960\n\t"
    "s_add_u32 m0, s60, 5120\n\t"
    "s_nop 0\n\t"
    "buffer_load_dwordx4 v17, %[rq], s61 offen lds\n\t"
    "s_add_u32 s61, s86, 3407872\n\t"
    "s_add_u32 m0, s60, 6144\n\t"
    "s_nop 0\n\t"
    "buffer_load_dwordx4 v17, %[rq], s61 offen lds\n\t"
    "s_add_u32 s61, s86, 3424256\n\t"
    "s_add_u32 m0, s60, 7168\n\t"
    "s_nop 0\n\t"
    "buffer_load_dwordx4 v17, %[rq], s61 offen lds\n\t"
    "s_waitcnt vmcnt(24)\n\t"
    "ds_read_b64_tr_b16 a[176:177], v77 offset:12288\n\t"
    "ds_read_b64_tr_b16 a[178:179], v77 offset:13312\n\t"
    "ds_read_b64_tr_b16 a[180:181], v77 offset:14336\n\t"
    "ds_read_b64_tr_b16 a[182:183], v77 offset:15360\n\t"
    "ds_read_b64_tr_b16 a[184:185], v78 offset:12288\n\t"
    "ds_read_b64_tr_b16 a[186:187], v78 offset:13312\n\t"
    "ds_read_b64_tr_b16 a[188:189], v78 offset:14336\n\t"
    "s_waitcnt lgkmcnt(14)\n\t"
    "ds_read_b64_tr_b16 a[190:191], v78 offset:15360\n\t"
    "s_waitcnt lgkmcnt(8)\n\t"
    "s_add_u32 s61, s80, 0\n\t"
    "s_add_u32 m0, s60, 8192\n\t"
    "s_nop 0\n\t"
    "buffer_load_dwordx4 v18, %[ro], s61 offen lds\n\t"
    "s_add_u32 s61, s80, 9216\n\t"
    "s_add_u32 m0, s60, 9216\n\t"
    "s_nop 0\n\t"
    "buffer_load_dwordx4 v18, %[ro], s61 offen lds\n\t"
    "s_add_u32 s61, s81, 0\n\t"
    "s_add_u32 m0, s60, 10240\n\t"
    "s_nop 0\n\t"
    "buffer_load_dwordx4 v18, %[ro], s61 offen lds\n\t"
    "s_add_u32 s61, s81, 9216\n\t"
    "s_add_u32 m0, s60, 11264\n\t"
    "s_nop 0\n\t"
    "buffer_load_dwordx4 v18, %[ro], s61 offen lds\n\t"
    "s_waitcnt vmcnt(24)\n\t"
    "ds_read_b64_tr_b16 a[192:193], v77 offset:16384\n\t"
    "ds_read_b64_tr_b16 a[194:195], v77 offset:17408\n\t"
    "ds_read_b64_tr_b16 a[196:197], v77 offset:18432\n\t"
    "ds_read_b64_tr_b16 a[198:199], v77 offset:19456\n\t"
    "ds_read_b64_tr_b16 a[200:201], v78 offset:16384\n\t"
    "ds_read_b64_tr_b16 a[202:203], v78 offset:17408\n\t"
    "ds_read_b64_tr_b16 a[204:205], v78 offset:18432\n\t"
    "s_waitcnt lgkmcnt(14)\n\t"
    "ds_read_b64_tr_b16 a[206:207], v78 offset:19456\n\t"
    "s_waitcnt lgkmcnt(8)\n\t"
    "s_add_u32 s61, s82, 0\n\t"
    "s_add_u32 m0, s60, 12288\n\t"
    "s_nop 0\n\t"
    "buffer_load_dwordx4 v18, %[ro], s61 offen lds\n\t"
    "s_add_u32 s61, s82, 9216\n\t"
    "s_add_u32 m0, s60, 13312\n\t"
    "s_nop 0\n\t"
    "buffer_load_dwordx4 v18, %[ro], s61 offen lds\n\t"
    "s_add_u32 s61, s83, 0\n\t"
    "s_add_u32 m0, s60, 14336\n\t"
    "s_nop 0\n\t"
    "buffer_load_dwordx4 v18, %[ro], s61 offen lds\n\t"
    "s_add_u32 s61, s83, 9216\n\t"
    "s_add_u32 m0, s60, 15360\n\t"
    "s_nop 0\n\t"
    "buffer_load_dwordx4 v18, %[ro], s61 offen lds\n\t"
    "s_waitcnt vmcnt(24)\n\t"
    "ds_read_b64_tr_b16 a[208:209], v77 offset:20480\n\t"
    "ds_read_b64_tr_b16 a[210:211], v77 offset:21504\n\t"
    "ds_read_b64_tr_b16 a[212:213], v77 offset:22528\n\t"
    "ds_read_b64_tr_b16 a[214:215], v77 offset:23552\n\t"
    "ds_read_b64_tr_b16 a[216:217], v78 offset:20480\n\t"
    "ds_read_b64_tr_b16 a[218:219], v78 offset:21504\n\t"
    "ds_read_b64_tr_b16 a[220:221], v78 offset:22528\n\t"
    "s_waitcnt lgkmcnt(14)\n\t"
    "ds_read_b64_tr_b16 a[222:223], v78 offset:23552\n\t"
    "s_waitcnt lgkmcnt(8)\n\t"
    "s_add_u32 s61, s80, 64\n\t"
    "s_add_u32 m0, s60, 16384\n\t"
    "s_nop 0\n\t"
    "buffer_load_dwordx4 v18, %[ro], s61 offen lds\n\t"
    "s_add_u32 s61, s80, 9280\n\t"
    "s_add_u32 m0, s60, 17408\n\t"
    "s_nop 0\n\t"
    "buffer_load_dwordx4 v18, %[ro], s61 offen lds\n\t"
    "s_add_u32 s61, s81, 64\n\t"
    "s_add_u32 m0, s60, 18432\n\t"
    "s_nop 0\n\t"
    "buffer_load_dwordx4 v18, %[ro], s61 offen lds\n\t"
    "s_add_u32 s61, s81, 9280\n\t"
    "s_add_u32 m0, s60, 19456\n\t"
    "s_nop 0\n\t"
    "buffer_load_dwordx4 v18, %[ro], s61 offen lds\n\t"
    "s_waitcnt vmcnt(24)\n\t"
    "ds_read_b64_tr_b16 a[224:225], v77 offset:24576\n\t"
    "ds_read_b64_tr_b16 a[226:227], v77 offset:25600\n\t"
    "ds_read_b64_tr_b16 a[228:229], v77 offset:26624\n\t"
    "ds_read_b64_tr_b16 a[230:231], v77 offset:27648\n\t"
    "ds_read_b64_tr_b16 a[232:233], v78 offset:24576\n\t"
    "ds_read_b64_tr_b16 a[234:235], v78 offset:25600\n\t"
    "ds_read_b64_tr_b16 a[236:237], v78 offset:26624\n\t"
    "s_waitcnt lgkmcnt(14)\n\t"
    "ds_read_b64_tr_b16 a[238:239], v78 offset:27648\n\t"
    "s_waitcnt lgkmcnt(8)\n\t"
    "s_add_u32 s61, s82, 64\n\t"
    "s_add_u32 m0, s60, 20480\n\t"
    "s_nop 0\n\t"
    "buffer_load_dwordx4 v18, %[ro], s61 offen lds\n\t"
    "s_add_u32 s61, s82, 9280\n\t"
    "s_add_u32 m0, s60, 21504\n\t"
    "s_nop 0\n\t"
    "buffer_load_dwordx4 v18, %[ro], s61 offen lds\n\t"
    "s_add_u32 s61, s83, 64\n\t"
    "s_add_u32 m0, s60, 22528\n\t"
    "s_nop 0\n\t"
    "buffer_load_dwordx4 v18, %[ro], s61 offen lds\n\t"
    "s_add_u32 s61, s83, 9280\n\t"
    "s_add_u32 m0, s60, 23552\n\t"
    "s_nop 0\n\t"
    "buffer_load_dwordx4 v18, %[ro], s61 offen lds\n\t"
    "s_waitcnt vmcnt(24)\n\t"
    "ds_read_b64_tr_b16 a[240:241], v77 offset:28672\n\t"
    "ds_read_b64_tr_b16 a[242:243], v77 offset:29696\n\t"
    "ds_read_b64_tr_b16 a[244:245], v77 offset:30720\n\t"
    "ds_read_b64_tr_b16 a[246:247], v77 offset:31744\n\t"
    "ds_read_b64_tr_b16 a[248:249], v78 offset:28672\n\t"
    "ds_read_b64_tr_b16 a[250:251], v78 offset:29696\n\t"
    "ds_read_b64_tr_b16 a[252:253], v78 offset:30720\n\t"
    "s_waitcnt lgkmcnt(14)\n\t"
    "ds_read_b64_tr_b16 a[254:255], v78 offset:31744\n\t"
    "s_waitcnt lgkmcnt(8)\n\t"
    "s_add_u32 s61, s80, 128\n\t"
    "s_add_u32 m0, s60, 24576\n\t"
    "s_nop 0\n\t"
    "buffer_load_dwordx4 v18, %[ro], s61 offen lds\n\t"
    "s_add_u32 s61, s80, 9344\n\t"
    "s_add_u32 m0, s60, 25600\n\t"
    "s_nop 0\n\t"
    "buffer_load_dwordx4 v18, %[ro], s61 offen lds\n\t"
    "s_add_u32 s61, s81, 128\n\t"
    "s_add_u32 m0, s60, 26624\n\t"
    "s_nop 0\n\t"
    "buffer_load_dwordx4 v18, %[ro], s61 offen lds\n\t"
    "s_add_u32 s61, s81, 9344\n\t"
    "s_add_u32 m0, s60, 27648\n\t"
    "s_nop 0\n\t"
    "buffer_load_dwordx4 v18, %[ro], s61 offen lds\n\t"
    "buffer_load_dword v72, off, %[rw], s98 sc1\n\t"
    "s_waitcnt vmcnt(25)\n\t"
    "ds_read_b64_tr_b16 v[80:81], v77 offset:0\n\t"
    "ds_read_b64_tr_b16 v[82:83], v77 offset:1024\n\t"
    "ds_read_b64_tr_b16 v[84:85], v77 offset:2048\n\t"
    "ds_read_b64_tr_b16 v[86:87], v77 offset:3072\n\t"
    "ds_read_b64_tr_b16 v[88:89], v78 offset:0\n\t"
    "ds_read_b64_tr_b16 v[90:91], v78 offset:1024\n\t"
    "ds_read_b64_tr_b16 v[92:93], v78 offset:2048\n\t"
    "s_waitcnt lgkmcnt(14)\n\t"
    "ds_read_b64_tr_b16 v[94:95], v78 offset:3072\n\t"
    "s_waitcnt lgkmcnt(8)\n\t"
    "s_add_u32 s61, s82, 128\n\t"
    "s_add_u32 m0, s60, 28672\n\t"
    "s_nop 0\n\t"
    "buffer_load_dwordx4 v18, %[ro], s61 offen lds\n\t"
    "s_add_u32 s61, s82, 9344\n\t"
    "s_add_u32 m0, s60, 29696\n\t"
    "s_nop 0\n\t"
    "buffer_load_dwordx4 v18, %[ro], s61 offen lds\n\t"
    "s_add_u32 s61, s83, 128\n\t"
    "s_add_u32 m0, s60, 30720\n\t"
    "s_nop 0\n\t"
    "buffer_load_dwordx4 v18, %[ro], s61 offen lds\n\t"
    "s_add_u32 s61, s83, 9344\n\t"
    "s_add_u32 m0, s60, 31744\n\t"
    "s_nop 0\n\t"
    "buffer_load_dwordx4 v18, %[ro], s61 offen lds\n\t"
    "s_waitcnt vmcnt(25)\n\t"
    "ds_read_b64_tr_b16 v[96:97], v77 offset:4096\n\t"
    "ds_read_b64_tr_b16 v[98:99], v77 offset:5120\n\t"
    "ds_read_b64_tr_b16 v[100:101], v77 offset:6144\n\t"
    "ds_read_b64_tr_b16 v[102:103], v77 offset:7168\n\t"
    "ds_read_b64_tr_b16 v[104:105], v78 offset:4096\n\t"
    "ds_read_b64_tr_b16 v[106:107], v78 offset:5120\n\t"
    "ds_read_b64_tr_b16 v[108:109], v78 offset:6144\n\t"
    "s_waitcnt lgkmcnt(14)\n\t"
    "ds_read_b64_tr_b16 v[110:111], v78 offset:7168\n\t"
    "s_waitcnt lgkmcnt(8)\n\t"
    "s_add_u32 s61, s80, 192\n\t"
    "s_add_u32 m0, s60, 0\n\t"
    "s_nop 0\n\t"
    "buffer_load_dwordx4 v18, %[ro], s61 offen lds\n\t"
    "s_add_u32 s61, s80, 9408\n\t"
    "s_add_u32 m0, s60, 1024\n\t"
    "s_nop 0\n\t"
    "buffer_load_dwordx4 v18, %[ro], s61 offen lds\n\t"
    "s_add_u32 s61, s81, 192\n\t"
    "s_add_u32 m0, s60, 2048\n\t"
    "s_nop 0\n\t"
    "buffer_load_dwordx4 v18, %[ro], s61 offen lds\n\t"
    "s_add_u32 s61, s81, 9408\n\t"
    "s_add_u32 m0, s60, 3072\n\t"
    "s_nop 0\n\t"
    "buffer_load_dwordx4 v18, %[ro], s61 offen lds\n\t"
    "s_waitcnt vmcnt(25)\n\t"
    "ds_read_b64_tr_b16 v[112:113], v79 offset:8192\n\t"
    "ds_read_b64_tr_b16 v[114:115], v79 offset:8704\n\t"
    "ds_read_b64_tr_b16 v[116:117], v79 offset:9216\n\t"
    "ds_read_b64_tr_b16 v[118:119], v79 offset:9728\n\t"
    "ds_read_b64_tr_b16 v[120:121], v79 offset:10240\n\t"
    "ds_read_b64_tr_b16 v[122:123], v79 offset:10752\n\t"
    "ds_read_b64_tr_b16 v[124:125], v79 offset:11264\n\t"
    "s_waitcnt lgkmcnt(14)\n\t"
    "ds_read_b64_tr_b16 v[126:127], v79 offset:11776\n\t"
    "s_waitcnt lgkmcnt(8)\n\t"
    "s_add_u32 s61, s82, 192\n\t"
    "s_add_u32 m0, s60, 4096\n\t"
    "s_nop 0\n\t"
    "buffer_load_dwordx4 v18, %[ro], s61 offen lds\n\t"
    "s_add_u32 s61, s82, 9408\n\t"
    "s_add_u32 m0, s60, 5120\n\t"
    "s_nop 0\n\t"
    "buffer_load_dwordx4 v18, %[ro], s61 offen lds\n\t"
    "s_add_u32 s61, s83, 192\n\t"
    "s_add_u32 m0, s60, 6144\n\t"
    "s_nop 0\n\t"
    "buffer_load_dwordx4 v18, %[ro], s61 offen lds\n\t"
    "s_add_u32 s61, s83, 9408\n\t"
    "s_add_u32 m0, s60, 7168\n\t"
    "s_nop 0\n\t"
    "buffer_load_dwordx4 v18, %[ro], s61 offen lds\n\t"
    "s_waitcnt vmcnt(25)\n\t"
    "ds_read_b64_tr_b16 v[128:129], v79 offset:12288\n\t"
    "ds_read_b64_tr_b16 v[130:131], v79 offset:12800\n\t"
    "ds_read_b64_tr_b16 v[132:133], v79 offset:13312\n\t"
    "ds_read_b64_tr_b16 v[134:135], v79 offset:13824\n\t"
    "ds_read_b64_tr_b16 v[136:137], v79 offset:14336\n\t"
    "ds_read_b64_tr_b16 v[138:139], v79 offset:14848\n\t"
    "ds_read_b64_tr_b16 v[140:141], v79 offset:15360\n\t"
    "s_waitcnt lgkmcnt(14)\n\t"
    "ds_read_b64_tr_b16 v[142:143], v79 offset:15872\n\t"
    "s_waitcnt lgkmcnt(8)\n\t"
    "s_add_u32 s61, s80, 256\n\t"
    "s_add_u32 m0, s60, 8192\n\t"
    "s_nop 0\n\t"
    "buffer_load_dwordx4 v18, %[ro], s61 offen lds\n\t"
    "s_add_u32 s61, s80, 9472\n\t"
    "s_add_u32 m0, s60, 9216\n\t"
    "s_nop 0\n\t"
    "buffer_load_dwordx4 v18, %[ro], s61 offen lds\n\t"
    "s_add_u32 s61, s81, 256\n\t"
    "s_add_u32 m0, s60, 10240\n\t"
    "s_nop 0\n\t"
    "buffer_load_dwordx4 v18, %[ro], s61 offen lds\n\t"
    "s_add_u32 s61, s81, 9472\n\t"
    "s_add_u32 m0, s60, 11264\n\t"
    "s_nop 0\n\t"
    "buffer_load_dwordx4 v18, %[ro], s61 offen lds\n\t"
    "s_waitcnt vmcnt(25)\n\t"
    "ds_read_b64_tr_b16 v[144:145], v79 offset:16384\n\t"
    "ds_read_b64_tr_b16 v[146:147], v79 offset:16896\n\t"
    "ds_read_b64_tr_b16 v[148:149], v79 offset:17408\n\t"
    "ds_read_b64_tr_b16 v[150:151], v79 offset:17920\n\t"
    "ds_read_b64_tr_b16 v[152:153], v79 offset:18432\n\t"
    "ds_read_b64_tr_b16 v[154:155], v79 offset:18944\n\t"
    "ds_read_b64_tr_b16 v[156:157], v79 offset:19456\n\t"
    "s_waitcnt lgkmcnt(14)\n\t"
    "ds_read_b64_tr_b16 v[158:159], v79 offset:19968\n\t"
    "s_waitcnt lgkmcnt(8)\n\t"
    "s_add_u32 s61, s82, 256\n\t"
    "s_add_u32 m0, s60, 12288\n\t"
    "s_nop 0\n\t"
    "buffer_load_dwordx4 v18, %[ro], s61 offen lds\n\t"
    "s_add_u32 s61, s82, 9472\n\t"
    "s_add_u32 m0, s60, 13312\n\t"
    "s_nop 0\n\t"
    "buffer_load_dwordx4 v18, %[ro], s61 offen lds\n\t"
    "s_add_u32 s61, s83, 256\n\t"
    "s_add_u32 m0, s60, 14336\n\t"
    "s_nop 0\n\t"
    "buffer_load_dwordx4 v18, %[ro], s61 offen lds\n\t"
    "s_add_u32 s61, s83, 9472\n\t"
    "s_add_u32 m0, s60, 15360\n\t"
    "s_nop 0\n\t"
    "buffer_load_dwordx4 v18, %[ro], s61 offen lds\n\t"
    "s_waitcnt vmcnt(25)\n\t"
    "ds_read_b64_tr_b16 v[160:161], v79 offset:20480\n\t"
    "ds_read_b64_tr_b16 v[162:163], v79 offset:20992\n\t"
    "ds_read_b64_tr_b16 v[164:165], v79 offset:21504\n\t"
    "ds_read_b64_tr_b16 v[166:167], v79 offset:22016\n\t"
    "ds_read_b64_tr_b16 v[168:169], v79 offset:22528\n\t"
    "ds_read_b64_tr_b16 v[170:171], v79 offset:23040\n\t"
    "ds_read_b64_tr_b16 v[172:173], v79 offset:23552\n\t"
    "s_waitcnt lgkmcnt(14)\n\t"
    "ds_read_b64_tr_b16 v[174:175], v79 offset:24064\n\t"
    "s_waitcnt vmcnt(21)\n\t"
    "s_waitcnt lgkmcnt(14)\n\t"
    "ds_read_b64_tr_b16 v[176:177], v79 offset:24576\n\t"
    "s_waitcnt lgkmcnt(14)\n\t"
    "ds_read_b64_tr_b16 v[178:179], v79 offset:25088\n\t"
    "s_waitcnt lgkmcnt(14)\n\t"
    "ds_read_b64_tr_b16 v[180:181], v79 offset:25600\n\t"
    "s_waitcnt lgkmcnt(14)\n\t"
    "ds_read_b64_tr_b16 v[182:183], v79 offset:26112\n\t"
    "s_waitcnt lgkmcnt(14)\n\t"
    "ds_read_b64_tr_b16 v[184:185], v79 offset:26624\n\t"
    "s_waitcnt lgkmcnt(14)\n\t"
    "ds_read_b64_tr_b16 v[186:187], v79 offset:27136\n\t"
    "s_waitcnt lgkmcnt(14)\n\t"
    "ds_read_b64_tr_b16 v[188:189], v79 offset:27648\n\t"
    "s_waitcnt lgkmcnt(14)\n\t"
    "ds_read_b64_tr_b16 v[190:191], v79 offset:28160\n\t"
    "s_waitcnt vmcnt(16)\n\t"
    "s_waitcnt lgkmcnt(14)\n\t"
    "ds_read_b64_tr_b16 v[192:193], v79 offset:28672\n\t"
    "s_waitcnt lgkmcnt(14)\n\t"
    "ds_read_b64_tr_b16 v[194:195], v79 offset:29184\n\t"
    "s_waitcnt lgkmcnt(14)\n\t"
    "ds_read_b64_tr_b16 v[196:197], v79 offset:29696\n\t"
    "s_waitcnt lgkmcnt(14)\n\t"
    "ds_read_b64_tr_b16 v[198:199], v79 offset:30208\n\t"
    "s_waitcnt lgkmcnt(14)\n\t"
    "ds_read_b64_tr_b16 v[200:201], v79 offset:30720\n\t"
    "s_waitcnt lgkmcnt(14)\n\t"
    "ds_read_b64_tr_b16 v[202:203], v79 offset:31232\n\t"
    "s_waitcnt lgkmcnt(14)\n\t"
    "ds_read_b64_tr_b16 v[204:205], v79 offset:31744\n\t"
    "s_waitcnt lgkmcnt(14)\n\t"
    "ds_read_b64_tr_b16 v[206:207], v79 offset:32256\n\t"
    "v_readfirstlane_b32 s97, v72\n\t"
    "s_mov_b32 s62, 2097536\n\t"
    "buffer_load_dword v73, off, %[rw], s62 sc1\n\t"
    "s_waitcnt vmcnt(13)\n\t"
    "s_waitcnt lgkmcnt(14)\n\t"
    "ds_read_b64_tr_b16 v[208:209], v79 offset:0\n\t"
    "s_waitcnt lgkmcnt(14)\n\t"
    "ds_read_b64_tr_b16 v[210:211], v79 offset:512\n\t"
    "s_waitcnt lgkmcnt(14)\n\t"
    "ds_read_b64_tr_b16 v[212:213], v79 offset:1024\n\t"
    "s_waitcnt lgkmcnt(14)\n\t"
    "ds_read_b64_tr_b16 v[214:215], v79 offset:1536\n\t"
    "s_waitcnt lgkmcnt(14)\n\t"
    "ds_read_b64_tr_b16 v[216:217], v79 offset:2048\n\t"
    "s_waitcnt lgkmcnt(14)\n\t"
    "ds_read_b64_tr_b16 v[218:219], v79 offset:2560\n\t"
    "s_waitcnt lgkmcnt(14)\n\t"
    "ds_read_b64_tr_b16 v[220:221], v79 offset:3072\n\t"
    "s_waitcnt lgkmcnt(14)\n\t"
    "ds_read_b64_tr_b16 v[222:223], v79 offset:3584\n\t"
    "s_waitcnt vmcnt(9)\n\t"
    "s_waitcnt lgkmcnt(14)\n\t"
    "ds_read_b64_tr_b16 v[224:225], v79 offset:4096\n\t"
    "s_waitcnt lgkmcnt(14)\n\t"
    "ds_read_b64_tr_b16 v[226:227], v79 offset:4608\n\t"
    "s_waitcnt lgkmcnt(14)\n\t"
    "ds_read_b64_tr_b16 v[228:229], v79 offset:5120\n\t"
    "s_waitcnt lgkmcnt(14)\n\t"
    "ds_read_b64_tr_b16 v[230:231], v79 offset:5632\n\t"
    "s_waitcnt lgkmcnt(14)\n\t"
    "ds_read_b64_tr_b16 v[232:233], v79 offset:6144\n\t"
    "s_waitcnt lgkmcnt(14)\n\t"
    "ds_read_b64_tr_b16 v[234:235], v79 offset:6656\n\t"
    "s_waitcnt lgkmcnt(14)\n\t"
    "ds_read_b64_tr_b16 v[236:237], v79 offset:7168\n\t"
    "s_waitcnt lgkmcnt(14)\n\t"
    "ds_read_b64_tr_b16 v[238:239], v79 offset:7680\n\t"
    "s_waitcnt vmcnt(5)\n\t"
    "s_waitcnt lgkmcnt(14)\n\t"
    "ds_read_b64_tr_b16 v[240:241], v79 offset:8192\n\t"
    "s_waitcnt lgkmcnt(14)\n\t"
    "ds_read_b64_tr_b16 v[242:243], v79 offset:8704\n\t"
    "s_waitcnt lgkmcnt(14)\n\t"
    "ds_read_b64_tr_b16 v[244:245], v79 offset:9216\n\t"
    "s_waitcnt lgkmcnt(14)\n\t"
    "ds_read_b64_tr_b16 v[246:247], v79 offset:9728\n\t"
    "s_waitcnt lgkmcnt(14)\n\t"
    "ds_read_b64_tr_b16 v[248:249], v79 offset:10240\n\t"
    "s_waitcnt lgkmcnt(14)\n\t"
    "ds_read_b64_tr_b16 v[250:251], v79 offset:10752\n\t"
    "s_waitcnt lgkmcnt(14)\n\t"
    "ds_read_b64_tr_b16 v[252:253], v79 offset:11264\n\t"
    "s_waitcnt lgkmcnt(14)\n\t"
    "ds_read_b64_tr_b16 v[254:255], v79 offset:11776\n\t"
    "s_waitcnt vmcnt(1)\n\t"
    "s_waitcnt lgkmcnt(14)\n\t"
    "ds_read_b64_tr_b16 v[28:29], v79 offset:12288\n\t"
    "s_waitcnt lgkmcnt(14)\n\t"
    "ds_read_b64_tr_b16 v[30:31], v79 offset:12800\n\t"
    "s_waitcnt lgkmcnt(14)\n\t"
    "ds_read_b64_tr_b16 v[32:33], v79 offset:13312\n\t"
    "s_waitcnt lgkmcnt(14)\n\t"
    "ds_read_b64_tr_b16 v[34:35], v79 offset:13824\n\t"
    "s_waitcnt lgkmcnt(14)\n\t"
    "ds_read_b64_tr_b16 v[36:37], v79 offset:14336\n\t"
    "s_waitcnt lgkmcnt(14)\n\t"
    "ds_read_b64_tr_b16 v[38:39], v79 offset:14848\n\t"
    "s_waitcnt lgkmcnt(14)\n\t"
    "ds_read_b64_tr_b16 v[40:41], v79 offset:15360\n\t"
    "s_waitcnt lgkmcnt(14)\n\t"
    "ds_read_b64_tr_b16 v[42:43], v79 offset:15872\n\t"
    "s_waitcnt lgkmcnt(0)\n\t"
    "s_add_u32 s94, s60, 28672\n\t"
    "s_sub_u32 s94, s94, 131072\n\t"
    "v_add_u32 v12, s94, v0\n\t"
    "ds_write_b128 v12, v[28:31] offset:0\n\t"
    "ds_write_b128 v12, v[32:35] offset:1024\n\t"
    "ds_write_b128 v12, v[36:39] offset:2048\n\t"
    "ds_write_b128 v12, v[40:43] offset:3072\n\t"
    "s_waitcnt lgkmcnt(0)\n\t"
    "s_mov_b32 s70, 0x200000\n\t"
    "s_waitcnt vmcnt(0)\n\t"
    "v_readfirstlane_b32 s95, v73\n\t"
    "s_cmp_lg_u32 s97, s99\n\t"
    "s_cbranch_scc1 LHS_%=\n\t"
    "s_cmp_lg_u32 s95, 0\n\t"
    "s_cbranch_scc1 LSED_%=\n\t"
    "s_branch LHSD_%=\n\t"
    "LHS_%=:\n\t"
    "buffer_load_dword v72, off, %[rw], s98 sc1\n\t"
    "s_waitcnt vmcnt(0)\n\t"
    "v_readfirstlane_b32 s95, v72\n\t"
    "s_cmp_eq_u32 s95, s99\n\t"
    "s_cbranch_scc1 LHSD_%=\n\t"
    "s_sleep 2\n\t"
    "s_sub_u32 s70, s70, 1\n\t"
    "s_cmp_lg_u32 s70, 0\n\t"
    "s_cbranch_scc1 LHS_%=\n\t"
    "s_branch LOUT_%=\n\t"
    "LHSD_%=:\n\t"
    "s_mov_b32 s98, 2097536\n\t"
    "LSE_%=:\n\t"
    "buffer_load_dword v72, off, %[rw], s98 sc1\n\t"
    "s_waitcnt vmcnt(0)\n\t"
    "v_readfirstlane_b32 s95, v72\n\t"
    "s_cmp_lg_u32 s95, 0\n\t"
    "s_cbranch_scc1 LSED_%=\n\t"
    "s_sleep 2\n\t"
    "s_sub_u32 s70, s70, 1\n\t"
    "s_cmp_lg_u32 s70, 0\n\t"
    "s_cbranch_scc1 LSE_%=\n\t"
    "s_branch LEND_%=\n\t"
    "LSED_%=:\n\t"
    "s_sub_u32 s95, s95, 1\n\t"
    "s_lshl_b32 s95, s95, 13\n\t"
    "s_lshl_b32 s98, %[slot], 2\n\t"
    "s_add_u32 s98, s98, 2113536\n\t"
    "s_mov_b32 s63, 0\n\t"
    "s_mov_b32 s77, 0x3e0293ee\n\t"
    "s_mov_b32 s90, 0x3e0293ee\n\t"
    "s_mov_b32 s91, 0x3e0293ee\n\t"
    "s_mov_b32 s84, 0x3f803f80\n\t"
    "s_mov_b32 s85, 0x3f803f80\n\t"
    "s_lshl_b32 s68, %[h], 13\n\t"
    "s_add_u32 s68, s68, 4063232\n\t"
    "s_add_u32 s78, s68, 16777216\n\t"
    "s_lshl_b32 s69, %[h], 12\n\t"
    "s_lshl_b32 s61, %[w], 10\n\t"
    "s_add_u32 s69, s69, s61\n\t"
    "s_add_u32 s69, s69, 37617664\n\t"
    "s_lshr_b32 s92, %[h], 1\n\t"
    "s_lshl_b32 s92, s92, 14\n\t"
    "s_and_b32 s62, %[h], 1\n\t"
    "s_lshl_b32 s62, s62, 12\n\t"
    "s_add_u32 s92, s92, s62\n\t"
    "s_add_u32 s92, s92, s61\n\t"
    "s_add_u32 s92, s92, s95\n\t"
    "s_add_u32 s92, s92, 1441792\n\t"
    "s_mov_b32 s93, 0\n\t"
    "s_lshl_b32 s72, %[h], 6\n\t"
    "s_add_u32 s73, s72, 2293760\n\t"
    "s_add_u32 s72, s72, 2162688\n\t"
    "s_lshl_b32 s61, %[w], 2\n\t"
    "s_add_u32 s72, s72, s61\n\t"
    "s_mov_b32 s75, 0\n\t"
    "s_mov_b32 s76, 0\n\t"
    "s_lshl_b32 s89, %[h], 7\n\t"
    "s_add_u32 s89, s89, s95\n\t"
    "buffer_load_dwordx2 v[76:77], off, %[rw], s73 sc1\n\t"
    "s_waitcnt vmcnt(0)\n\t"
    "LKVA_%=:\n\t"
    "s_nop 1\n\t"
    "v_min_u32 v76, v76, v77\n\t"
    "v_cmp_ne_u32 vcc, 0, v76\n\t"
    "s_cbranch_vccnz LKVDA_%=\n\t"
    "s_sleep 2\n\t"
    "buffer_load_dwordx2 v[76:77], off, %[rw], s73 sc1\n\t"
    "s_waitcnt vmcnt(0)\n\t"
    "s_sub_u32 s70, s70, 1\n\t"
    "s_cmp_lg_u32 s70, 0\n\t"
    "s_cbranch_scc1 LKVA_%=\n\t"
    "s_branch LEND_%=\n\t"
    "LKVDA_%=:\n\t"
    "s_add_u32 s73, s73, 2048\n\t"
    "s_add_u32 m0, s74, 140288\n\t"
    "s_add_u32 s79, s68, 0\n\t"
    "buffer_load_dwordx4 v2, %[rw], s79 offen sc1 lds\n\t"
    "s_add_u32 m0, s74, 141312\n\t"
    "s_add_u32 s79, s68, 1024\n\t"
    "buffer_load_dwordx4 v2, %[rw], s79 offen sc1 lds\n\t"
    "s_add_u32 m0, s74, 148480\n\t"
    "s_add_u32 s79, s78, 0\n\t"
    "buffer_load_dwordx4 v2, %[rw], s79 offen sc1 lds\n\t"
    "s_add_u32 m0, s74, 149504\n\t"
    "s_add_u32 s79, s78, 1024\n\t"
    "buffer_load_dwordx4 v2, %[rw], s79 offen sc1 lds\n\t"
    "s_add_u32 s68, s68, 262144\n\t"
    "s_add_u32 s78, s78, 262144\n\t"
    "buffer_load_dwordx2 v[76:77], off, %[rw], s73 sc1\n\t"
    "s_nop 7\n\t"
    "LTOP_%=:\n\t"
    "v_mov_b64 v[24:25], 0\n\t"
    "v_mov_b64 v[26:27], 0\n\t"
    "v_mov_b64 v[28:29], 0\n\t"
    "v_mov_b64 v[30:31], 0\n\t"
    "v_mov_b64 v[32:33], 0\n\t"
    "v_mov_b64 v[34:35], 0\n\t"
    "v_mov_b64 v[36:37], 0\n\t"
    "v_mov_b64 v[38:39], 0\n\t"
    "v_and_b32 v78, 240, v0\n\t"
    "s_lshl_b32 s61, %[w], 8\n\t"
    "s_add_u32 s61, s61, 139264\n\t"
    "v_add_u32 v78, s61, v78\n\t"
    "s_and_b32 s61, %[h], 3\n\t"
    "s_lshl_b32 s61, s61, 15\n\t"
    "s_add_u32 s66, s61, s95\n\t"
    "s_add_u32 s66, s66, s76\n\t"
    "s_add_u32 s66, s66, 1048576\n\t"
    "s_add_u32 s67, s66, 8192\n\t"
    "s_add_u32 s61, s89, s75\n\t"
    "s_lshl_b32 s62, %[w], 17\n\t"
    "s_add_u32 s64, s61, s62\n\t"
    "s_add_u32 s65, s64, 4096\n\t"
    "s_lshl_b32 s62, %[w], 14\n\t"
    "s_add_u32 s71, s61, s62\n\t"
    "s_add_u32 s71, s71, 524288\n\t"
    "s_add_u32 s88, s92, s93\n\t"
    "s_waitcnt vmcnt(0)\n\t"
    "LKVB_%=:\n\t"
    "s_nop 1\n\t"
    "v_min_u32 v76, v76, v77\n\t"
    "v_cmp_ne_u32 vcc, 0, v76\n\t"
    "s_cbranch_vccnz LKVDB_%=\n\t"
    "s_sleep 2\n\t"
    "buffer_load_dwordx2 v[76:77], off, %[rw], s73 sc1\n\t"
    "s_waitcnt vmcnt(0)\n\t"
    "s_sub_u32 s70, s70, 1\n\t"
    "s_cmp_lg_u32 s70, 0\n\t"
    "s_cbranch_scc1 LKVB_%=\n\t"
    "s_branch LEND_%=\n\t"
    "LKVDB_%=:\n\t"
    "s_mov_b32 exec_lo, 0x00010001\n\t"
    "s_mov_b32 exec_hi, 0x00010001\n\t"
    "LP_%=:\n\t"
    "s_nop 1\n\t"
    "buffer_load_dword v79, v2, %[rw], s66 offen offset:12 sc1\n\t"
    "s_waitcnt vmcnt(0)\n\t"
    "v_cmp_ne_u32 vcc, -1, v79\n\t"
    "s_cbranch_vccnz LPD_%=\n\t"
    "s_sleep 1\n\t"
    "s_sub_u32 s70, s70, 1\n\t"
    "s_cmp_lg_u32 s70, 0\n\t"
    "s_cbranch_scc1 LP_%=\n\t"
    "LPD_%=:\n\t"
    "s_mov_b64 exec, -1\n\t"
    "LG_%=:\n\t"
    "s_nop 1\n\t"
    "buffer_load_dwordx4 v[12:15], v2, %[rw], s66 offen offset:0 sc1\n\t"
    "buffer_load_dwordx4 v[16:19], v2, %[rw], s66 offen offset:1024 sc1\n\t"
    "buffer_load_dwordx4 v[20:23], v78, %[rw], s67 offen offset:0 sc1\n\t"
    "s_waitcnt vmcnt(2)\n\t"
    "v_max3_u32 v79, v12, v13, v14\n\t"
    "s_waitcnt vmcnt(1)\n\t"
    "v_max3_u32 v76, v15, v16, v17\n\t"
    "v_max3_u32 v79, v79, v18, v19\n\t"
    "s_waitcnt vmcnt(0)\n\t"
    "v_max3_u32 v76, v76, v20, v21\n\t"
    "v_max3_u32 v79, v79, v22, v23\n\t"
    "v_max_u32 v79, v79, v76\n\t"
    "v_cmp_eq_u32 vcc, -1, v79\n\t"
    "s_cbranch_vccz LGOK_%=\n\t"
    "s_sub_u32 s70, s70, 1\n\t"
    "s_cmp_lg_u32 s70, 0\n\t"
    "s_cbranch_scc1 LG_%=\n\t"
    "s_branch LEND_%=\n\t"
    "LGOK_%=:\n\t"
    "ds_write_b128 v2, v[12:15] offset:0\n\t"
    "ds_write_b128 v2, v[16:19] offset:1024\n\t"
    "ds_write_b128 v78, v[20:23] offset:0\n\t"
    "v_smfmac_f32_16x16x64_bf16 v[24:27], v[12:15], a[144:151], v1\n\t"
    "v_smfmac_f32_16x16x64_bf16 v[28:31], v[12:15], a[152:159], v1\n\t"
    "s_waitcnt lgkmcnt(0)\n\t"
    "s_barrier\n\t"
    "ds_read_b128 v[20:23], v9 offset:0\n\t"
    "ds_read_b128 v[12:15], v9 offset:1024\n\t"
    "v_smfmac_f32_16x16x64_bf16 v[24:27], v[16:19], a[160:167], v1\n\t"
    "v_smfmac_f32_16x16x64_bf16 v[28:31], v[16:19], a[168:175], v1\n\t"
    "ds_read_b128 v[16:19], v10 offset:0\n\t"
    "s_waitcnt lgkmcnt(2)\n\t"
    "v_smfmac_f32_16x16x64_bf16 v[24:27], v[20:23], a[176:183], v1\n\t"
    "ds_read_b128 v[40:43], v0 offset:9216\n\t"
    "v_smfmac_f32_16x16x64_bf16 v[28:31], v[20:23], a[184:191], v1\n\t"
    "ds_read_b128 v[20:23], v10 offset:1024\n\t"
    "s_waitcnt lgkmcnt(3)\n\t"
    "v_smfmac_f32_16x16x64_bf16 v[24:27], v[12:15], a[192:199], v1\n\t"
    "ds_read_b128 v[44:47], v0 offset:13312\n\t"
    "v_smfmac_f32_16x16x64_bf16 v[28:31], v[12:15], a[200:207], v1\n\t"
    "ds_read_b128 v[12:15], v11 offset:0\n\t"
    "s_waitcnt lgkmcnt(4)\n\t"
    "v_smfmac_f32_16x16x64_bf16 v[24:27], v[16:19], a[208:215], v1\n\t"
    "ds_read_b128 v[48:51], v0 offset:10240\n\t"
    "v_smfmac_f32_16x16x64_bf16 v[28:31], v[16:19], a[216:223], v1\n\t"
    "ds_read_b128 v[16:19], v11 offset:1024\n\t"
    "s_waitcnt lgkmcnt(4)\n\t"
    "v_smfmac_f32_16x16x64_bf16 v[24:27], v[20:23], a[224:231], v1\n\t"
    "ds_read_b128 v[52:55], v0 offset:14336\n\t"
    "v_smfmac_f32_16x16x64_bf16 v[28:31], v[20:23], a[232:239], v1\n\t"
    "ds_read_b128 v[20:23], v0 offset:8192\n\t"
    "s_waitcnt lgkmcnt(4)\n\t"
    "v_smfmac_f32_16x16x64_bf16 v[24:27], v[12:15], a[240:247], v1\n\t"
    "ds_read_b128 v[56:59], v0 offset:11264\n\t"
    "v_smfmac_f32_16x16x64_bf16 v[28:31], v[12:15], a[248:255], v1\n\t"
    "ds_read_b128 v[12:15], v2 offset:0\n\t"
    "s_waitcnt lgkmcnt(4)\n\t"
    "v_smfmac_f32_16x16x64_bf16 v[24:27], v[16:19], v[80:87], v1\n\t"
    "s_cmp_eq_u32 s63, 0\n\t"
    "s_cbranch_scc1 LNF_%=\n\t"
    "buffer_store_dword v1, off, %[rw], s72 sc1\n\t"
    "s_add_u32 s72, s72, 2048\n\t"
    "LNF_%=:\n\t"
    "v_smfmac_f32_16x16x64_bf16 v[28:31], v[16:19], v[88:95], v1\n\t"
    "ds_read_b128 v[16:19], v2 offset:1024\n\t"
    "s_waitcnt lgkmcnt(3)\n\t"
    "v_smfmac_f32_16x16x64_bf16 v[24:27], v[20:23], v[96:103], v1\n\t"
    "ds_read_b128 v[60:63], v0 offset:15360\n\t"
    "v_smfmac_f32_16x16x64_bf16 v[28:31], v[20:23], v[104:111], v1\n\t"
    "ds_read_b128 v[20:23], v9 offset:0\n\t"
    "s_waitcnt lgkmcnt(3)\n\t"
    "v_smfmac_f32_16x16x64_bf16 v[32:35], v[12:15], a[0:7], v1\n\t"
    "v_smfmac_f32_16x16x64_bf16 v[36:39], v[12:15], a[8:15], v1\n\t"
    "ds_read_b128 v[12:15], v9 offset:1024\n\t"
    "v_add_f32 v24, v24, v26\n\t"
    "s_waitcnt lgkmcnt(3)\n\t"
    "v_smfmac_f32_16x16x64_bf16 v[32:35], v[16:19], a[16:23], v1\n\t"
    "v_add_f32 v25, v25, v27\n\t"
    "v_cvt_pk_bf16_f32 v26, v24, v25\n\t"
    "v_smfmac_f32_16x16x64_bf16 v[36:39], v[16:19], a[24:31], v1\n\t"
    "ds_read_b128 v[16:19], v10 offset:0\n\t"
    "v_cvt_pk_bf16_f32 v27, v24, v25\n\t"
    "s_waitcnt lgkmcnt(2)\n\t"
    "v_smfmac_f32_16x16x64_bf16 v[32:35], v[20:23], a[32:39], v1\n\t"
    "v_add_f32 v28, v28, v30\n\t"
    "v_add_f32 v29, v29, v31\n\t"
    "v_smfmac_f32_16x16x64_bf16 v[36:39], v[20:23], a[40:47], v1\n\t"
    "ds_read_b128 v[20:23], v10 offset:1024\n\t"
    "v_cvt_pk_bf16_f32 v30, v28, v29\n\t"
    "s_waitcnt lgkmcnt(2)\n\t"
    "v_smfmac_f32_16x16x64_bf16 v[32:35], v[12:15], a[48:55], v1\n\t"
    "v_cvt_pk_bf16_f32 v31, v28, v29\n\t"
    "ds_write2_b64 v3, v[26:27], v[30:31] offset0:0 offset1:16\n\t"
    "v_smfmac_f32_16x16x64_bf16 v[36:39], v[12:15], a[56:63], v1\n\t"
    "ds_read_b128 v[12:15], v11 offset:0\n\t"
    "s_waitcnt lgkmcnt(3)\n\t"
    "v_smfmac_f32_16x16x64_bf16 v[32:35], v[16:19], a[64:71], v1\n\t"
    "ds_read_b128 v[64:67], v0 offset:12288\n\t"
    "v_smfmac_f32_16x16x64_bf16 v[36:39], v[16:19], a[72:79], v1\n\t"
    "ds_read_b128 v[16:19], v11 offset:1024\n\t"
    "s_waitcnt lgkmcnt(3)\n\t"
    "s_barrier\n\t"
    "v_smfmac_f32_16x16x64_bf16 v[32:35], v[20:23], a[80:87], v1\n\t"
    "ds_read_b64_tr_b16 v[24:25], v4 offset:0\n\t"
    "ds_read_b64_tr_b16 v[26:27], v4 offset:128\n\t"
    "v_smfmac_f32_16x16x64_bf16 v[36:39], v[20:23], a[88:95], v1\n\t"
    "ds_read_b128 v[20:23], v0 offset:8192\n\t"
    "ds_read_b64_tr_b16 v[28:29], v4 offset:256\n\t"
    "s_waitcnt lgkmcnt(6)\n\t"
    "v_smfmac_f32_16x16x64_bf16 v[32:35], v[12:15], a[96:103], v1\n\t"
    "ds_read_b64_tr_b16 v[30:31], v4 offset:384\n\t"
    "ds_read_b64_tr_b16 v[72:73], v4 offset:512\n\t"
    "v_smfmac_f32_16x16x64_bf16 v[36:39], v[12:15], a[104:111], v1\n\t"
    "ds_read_b64_tr_b16 v[74:75], v4 offset:640\n\t"
    "ds_read_b128 v[68:71], v0 offset:16384\n\t"
    "s_waitcnt lgkmcnt(8)\n\t"
    "v_smfmac_f32_16x16x64_bf16 v[32:35], v[16:19], a[112:119], v1\n\t"
    "ds_read_b64_tr_b16 v[76:77], v4 offset:768\n\t"
    "v_smfmac_f32_16x16x64_bf16 v[36:39], v[16:19], a[120:127], v1\n\t"
    "ds_read_b64_tr_b16 v[78:79], v4 offset:896\n\t"
    "s_waitcnt lgkmcnt(7)\n\t"
    "v_smfmac_f32_16x16x64_bf16 v[32:35], v[20:23], a[128:135], v1\n\t"
    "v_smfmac_f32_16x16x64_bf16 v[36:39], v[20:23], a[136:143], v1\n\t"
    "v_mfma_f32_16x16x32_bf16 v[40:43], v[40:43], v[24:27], 0\n\t"
    "buffer_load_dwordx4 v[12:15], v0, %[rw], s88 offen offset:0 sc1\n\t"
    "v_mfma_f32_16x16x32_bf16 v[44:47], v[44:47], v[24:27], 0\n\t"
    "v_add_f32 v32, v32, v34\n\t"
    "v_add_f32 v33, v33, v35\n\t"
    "s_waitcnt lgkmcnt(5)\n\t"
    "v_mfma_f32_16x16x32_bf16 v[40:43], v[48:51], v[28:31], v[40:43]\n\t"
    "v_cvt_pk_bf16_f32 v34, v32, v33\n\t"
    "v_cvt_pk_bf16_f32 v35, v32, v33\n\t"
    "ds_write_b64 v3, v[34:35] offset:4608\n\t"
    "v_add_f32 v36, v36, v38\n\t"
    "v_add_f32 v37, v37, v39\n\t"
    "v_cvt_pk_bf16_f32 v38, v36, v37\n\t"
    "v_cvt_pk_bf16_f32 v39, v36, v37\n\t"
    "ds_write_b64 v3, v[38:39] offset:4736\n\t"
    "v_mfma_f32_16x16x32_bf16 v[44:47], v[52:55], v[28:31], v[44:47]\n\t"
    "s_waitcnt lgkmcnt(5)\n\t"
    "v_mfma_f32_16x16x32_bf16 v[40:43], v[56:59], v[72:75], v[40:43]\n\t"
    "s_waitcnt lgkmcnt(0)\n\t"
    "s_barrier\n\t"
    "ds_read_b64_tr_b16 v[16:17], v4 offset:4608\n\t"
    "ds_read_b64_tr_b16 v[18:19], v4 offset:4736\n\t"
    "ds_read_b64_tr_b16 v[20:21], v4 offset:4864\n\t"
    "ds_read_b64_tr_b16 v[22:23], v4 offset:4992\n\t"
    "v_mfma_f32_16x16x32_bf16 v[44:47], v[60:63], v[72:75], v[44:47]\n\t"
    "ds_read_b64_tr_b16 v[32:33], v4 offset:5120\n\t"
    "ds_read_b64_tr_b16 v[34:35], v4 offset:5248\n\t"
    "v_mfma_f32_16x16x32_bf16 v[40:43], v[64:67], v[76:79], v[40:43]\n\t"
    "ds_read_b64_tr_b16 v[36:37], v4 offset:5376\n\t"
    "ds_read_b64_tr_b16 v[38:39], v4 offset:5504\n\t"
    "v_mfma_f32_16x16x32_bf16 v[44:47], v[68:71], v[76:79], v[44:47]\n\t"
    "ds_read_b128 v[48:51], v2 offset:17408\n\t"
    "ds_read_b128 v[52:55], v2 offset:18432\n\t"
    "v_mov_b64 v[56:57], s[84:85]\n\t"
    "v_mov_b64 v[58:59], s[84:85]\n\t"
    "s_cmp_eq_u32 s63, 63\n\t"
    "s_cbranch_scc1 LLAST_%=\n\t"
    "s_waitcnt lgkmcnt(8)\n\t"
    "v_mfma_f32_16x16x32_bf16 v[16:19], v[16:19], v[24:27], 0\n\t"
    "v_max3_f32 v60, v40, v41, v42\n\t"
    "s_waitcnt lgkmcnt(6)\n\t"
    "v_mfma_f32_16x16x32_bf16 v[16:19], v[20:23], v[28:31], v[16:19]\n\t"
    "v_max3_f32 v61, v43, v44, v45\n\t"
    "s_waitcnt lgkmcnt(4)\n\t"
    "v_mfma_f32_16x16x32_bf16 v[16:19], v[32:35], v[72:75], v[16:19]\n\t"
    "v_max3_f32 v60, v60, v46, v47\n\t"
    "s_waitcnt lgkmcnt(2)\n\t"
    "v_mfma_f32_16x16x32_bf16 v[16:19], v[36:39], v[76:79], v[16:19]\n\t"
    "v_add_u32 v63, s94, v0\n\t"
    "v_mov_b64 v[64:65], 0\n\t"
    "v_mov_b64 v[66:67], 0\n\t"
    "v_mov_b64 v[68:69], 0\n\t"
    "v_mov_b64 v[70:71], 0\n\t"
    "v_mov_b64 v[20:21], 0\n\t"
    "s_nop 1\n\t"
    "v_max3_f32 v60, v60, v16, v17\n\t"
    "v_max_f32 v60, v60, v61\n\t"
    "v_mov_b32 v61, v60\n\t"
    "ds_read_b128 v[24:27], v63 offset:0\n\t"
    "ds_read_b128 v[28:31], v63 offset:1024\n\t"
    "v_permlane16_swap_b32 v60, v61\n\t"
    "v_max_f32 v60, v60, v61\n\t"
    "v_mov_b32 v61, v60\n\t"
    "ds_read_b128 v[72:75], v63 offset:2048\n\t"
    "ds_read_b128 v[76:79], v63 offset:3072\n\t"
    "v_permlane32_swap_b32 v60, v61\n\t"
    "v_max_f32 v60, v60, v61\n\t"
    "v_mul_f32 v60, s77, v60\n\t"
    "v_pk_fma_f32 v[40:41], v[40:41], s[90:91], v[60:61] op_sel_hi:[1,0,0] neg_lo:[0,0,1] neg_hi:[0,0,1]\n\t"
    "v_pk_fma_f32 v[42:43], v[42:43], s[90:91], v[60:61] op_sel_hi:[1,0,0] neg_lo:[0,0,1] neg_hi:[0,0,1]\n\t"
    "v_pk_fma_f32 v[44:45], v[44:45], s[90:91], v[60:61] op_sel_hi:[1,0,0] neg_lo:[0,0,1] neg_hi:[0,0,1]\n\t"
    "v_pk_fma_f32 v[46:47], v[46:47], s[90:91], v[60:61] op_sel_hi:[1,0,0] neg_lo:[0,0,1] neg_hi:[0,0,1]\n\t"
    "v_pk_fma_f32 v[16:17], v[16:17], s[90:91], v[60:61] op_sel_hi:[1,0,0] neg_lo:[0,0,1] neg_hi:[0,0,1]\n\t"
    "v_exp_f32 v40, v40\n\t"
    "v_exp_f32 v41, v41\n\t"
    "v_exp_f32 v42, v42\n\t"
    "v_exp_f32 v43, v43\n\t"
    "v_exp_f32 v44, v44\n\t"
    "v_cvt_pk_bf16_f32 v40, v40, v41\n\t"
    "v_exp_f32 v45, v45\n\t"
    "v_cvt_pk_bf16_f32 v41, v42, v43\n\t"
    "v_exp_f32 v46, v46\n\t"
    "v_exp_f32 v47, v47\n\t"
    "v_exp_f32 v16, v16\n\t"
    "v_cvt_pk_bf16_f32 v42, v44, v45\n\t"
    "v_cvt_pk_bf16_f32 v43, v46, v47\n\t"
    "v_exp_f32 v17, v17\n\t"
    "s_nop 0\n\t"
    "v_mfma_f32_16x16x32_bf16 v[60:63], v[56:59], v[40:43], 0\n\t"
    "v_cvt_pk_bf16_f32 v18, v16, v17\n\t"
    "v_cvt_pk_bf16_f32 v19, v16, v17\n\t"
    "s_waitcnt lgkmcnt(5)\n\t"
    "v_mfma_f32_16x16x32_bf16 v[48:51], v[48:51], v[40:43], 0\n\t"
    "v_mov_b64 v[16:17], 0\n\t"
    "s_waitcnt lgkmcnt(4)\n\t"
    "v_mfma_f32_16x16x32_bf16 v[52:55], v[52:55], v[40:43], 0\n\t"
    "v_mov_b64 v[22:23], 0\n\t"
    "v_mfma_f32_16x16x32_bf16 v[44:47], v[56:59], v[16:19], 0\n\t"
    "v_mov_b64 v[32:33], 0\n\t"
    "v_mov_b64 v[34:35], 0\n\t"
    "LV_%=:\n\t"
    "s_waitcnt vmcnt(0)\n\t"
    "v_max3_u32 v36, v12, v13, v14\n\t"
    "v_max_u32 v36, v36, v15\n\t"
    "v_cmp_eq_u32 vcc, -1, v36\n\t"
    "s_cbranch_vccz LVOK_%=\n\t"
    "buffer_load_dwordx4 v[12:15], v0, %[rw], s88 offen offset:0 sc1\n\t"
    "s_sub_u32 s70, s70, 1\n\t"
    "s_cmp_lg_u32 s70, 0\n\t"
    "s_cbranch_scc1 LV_%=\n\t"
    "s_branch LEND_%=\n\t"
    "LVOK_%=:\n\t"
    "v_mfma_f32_16x16x32_bf16 v[48:51], v[12:15], v[18:21], v[48:51]\n\t"
    "v_mfma_f32_16x16x32_bf16 v[52:55], v[12:15], v[16:19], v[52:55]\n\t"
    "v_fma_f32 v60, v44, 0.5, v60\n\t"
    "v_rcp_f32 v60, v60\n\t"
    "v_mov_b64 v[36:37], 0\n\t"
    "v_mov_b64 v[38:39], 0\n\t"
    "v_xor_b32 v40, 1024, v5\n\t"
    "v_pk_mul_f32 v[48:49], v[48:49], v[60:61] op_sel_hi:[1,0]\n\t"
    "v_pk_mul_f32 v[50:51], v[50:51], v[60:61] op_sel_hi:[1,0]\n\t"
    "s_nop 0\n\t"
    "v_pk_mul_f32 v[52:53], v[52:53], v[60:61] op_sel_hi:[1,0]\n\t"
    "v_pk_mul_f32 v[54:55], v[54:55], v[60:61] op_sel_hi:[1,0]\n\t"
    "v_cvt_pk_bf16_f32 v48, v48, v49\n\t"
    "v_cvt_pk_bf16_f32 v49, v50, v51\n\t"
    "v_cvt_pk_bf16_f32 v50, v52, v53\n\t"
    "v_cvt_pk_bf16_f32 v51, v54, v55\n\t"
    "ds_write_b128 v5, v[48:51] offset:0\n\t"
    "s_nop 0\n\t"
    "v_smfmac_f32_16x16x64_bf16 v[36:39], v[48:51], v[240:247], v1\n\t"
    "v_xor_b32 v41, 2048, v5\n\t"
    "v_smfmac_f32_16x16x64_bf16 v[64:67], v[48:51], v[112:119], v1\n\t"
    "v_xor_b32 v42, 3072, v5\n\t"
    "s_waitcnt lgkmcnt(0)\n\t"
    "s_barrier\n\t"
    "ds_read_b128 v[52:55], v40 offset:0\n\t"
    "ds_read_b128 v[56:59], v41 offset:0\n\t"
    "ds_read_b128 v[60:63], v42 offset:0\n\t"
    "v_smfmac_f32_16x16x64_bf16 v[68:71], v[48:51], v[144:151], v1\n\t"
    "v_smfmac_f32_16x16x64_bf16 v[20:23], v[48:51], v[176:183], v1\n\t"
    "v_smfmac_f32_16x16x64_bf16 v[32:35], v[48:51], v[208:215], v1\n\t"
    "s_waitcnt lgkmcnt(2)\n\t"
    "v_smfmac_f32_16x16x64_bf16 v[36:39], v[52:55], v[248:255], v1\n\t"
    "v_smfmac_f32_16x16x64_bf16 v[64:67], v[52:55], v[120:127], v1\n\t"
    "v_smfmac_f32_16x16x64_bf16 v[68:71], v[52:55], v[152:159], v1\n\t"
    "v_smfmac_f32_16x16x64_bf16 v[20:23], v[52:55], v[184:191], v1\n\t"
    "v_smfmac_f32_16x16x64_bf16 v[32:35], v[52:55], v[216:223], v1\n\t"
    "s_waitcnt lgkmcnt(1)\n\t"
    "v_smfmac_f32_16x16x64_bf16 v[36:39], v[56:59], v[24:31], v1\n\t"
    "s_waitcnt lgkmcnt(0)\n\t"
    "v_smfmac_f32_16x16x64_bf16 v[36:39], v[60:63], v[72:79], v1\n\t"
    "v_smfmac_f32_16x16x64_bf16 v[64:67], v[56:59], v[128:135], v1\n\t"
    "v_smfmac_f32_16x16x64_bf16 v[64:67], v[60:63], v[136:143], v1\n\t"
    "s_nop 1\n\t"
    "v_cvt_pk_bf16_f32 v36, v36, v37\n\t"
    "v_smfmac_f32_16x16x64_bf16 v[68:71], v[56:59], v[160:167], v1\n\t"
    "s_nop 0\n\t"
    "v_mov_b32_dpp v37, v36 quad_perm:[1,0,3,2] row_mask:0xf bank_mask:0xf\n\t"
    "v_smfmac_f32_16x16x64_bf16 v[68:71], v[60:63], v[168:175], v1\n\t"
    "v_perm_b32 v36, v37, v36, v8\n\t"
    "buffer_store_dword v36, v7, %[rw], s71 offen offset:0 sc1\n\t"
    "v_smfmac_f32_16x16x64_bf16 v[20:23], v[56:59], v[192:199], v1\n\t"
    "v_cvt_pk_bf16_f32 v44, v64, v66\n\t"
    "v_cvt_pk_bf16_f32 v16, v65, v67\n\t"
    "v_smfmac_f32_16x16x64_bf16 v[20:23], v[60:63], v[200:207], v1\n\t"
    "v_smfmac_f32_16x16x64_bf16 v[32:35], v[56:59], v[224:231], v1\n\t"
    "v_cvt_pk_bf16_f32 v45, v68, v70\n\t"
    "v_cvt_pk_bf16_f32 v17, v69, v71\n\t"
    "v_smfmac_f32_16x16x64_bf16 v[32:35], v[60:63], v[232:239], v1\n\t"
    "v_cvt_pk_bf16_f32 v46, v20, v22\n\t"
    "v_cvt_pk_bf16_f32 v18, v21, v23\n\t"
    "s_nop 5\n\t"
    "v_cvt_pk_bf16_f32 v47, v32, v34\n\t"
    "v_cvt_pk_bf16_f32 v19, v33, v35\n\t"
    "buffer_store_dwordx4 v[44:47], v6, %[rw], s64 offen offset:0 sc1\n\t"
    "buffer_store_dwordx4 v[16:19], v6, %[rw], s65 offen offset:0 sc1\n\t"
    "s_sleep 5\n\t"
    "s_lshl_b32 s61, %[w], 8\n\t"
    "v_add_u32 v40, s61, v4\n\t"
    "ds_read_b64_tr_b16 v[64:65], v40 offset:4608\n\t"
    "ds_read_b64_tr_b16 v[66:67], v40 offset:4736\n\t"
    "s_waitcnt lgkmcnt(0)\n\t"
    "buffer_store_dwordx4 v[64:67], v0, %[rw], s69 offen offset:0 sc1\n\t"
    "s_cmp_lg_u32 s63, 0\n\t"
    "s_cbranch_scc1 LNP_%=\n\t"
    "buffer_store_dword v1, off, %[rw], s98 sc1\n\t"
    "LNP_%=:\n\t"
    "v_mov_b64 v[68:69], -1\n\t"
    "v_mov_b64 v[70:71], -1\n\t"
    "buffer_store_dwordx4 v[68:71], v0, %[rw], s88 offen offset:0 sc1\n\t"
    "s_add_u32 s69, s69, 131072\n\t"
    "s_add_u32 s63, s63, 1\n\t"
    "s_xor_b32 s75, s75, 589824\n\t"
    "s_xor_b32 s93, s93, 262144\n\t"
    "s_add_u32 s76, s76, 131072\n\t"
    "s_cmp_eq_u32 s76, 393216\n\t"
    "s_cselect_b32 s76, 0, s76\n\t"
    "s_add_u32 m0, s74, 140288\n\t"
    "s_add_u32 s79, s68, 0\n\t"
    "buffer_load_dwordx4 v2, %[rw], s79 offen sc1 lds\n\t"
    "s_add_u32 m0, s74, 141312\n\t"
    "s_add_u32 s79, s68, 1024\n\t"
    "buffer_load_dwordx4 v2, %[rw], s79 offen sc1 lds\n\t"
    "s_add_u32 m0, s74, 148480\n\t"
    "s_add_u32 s79, s78, 0\n\t"
    "buffer_load_dwordx4 v2, %[rw], s79 offen sc1 lds\n\t"
    "s_add_u32 m0, s74, 149504\n\t"
    "s_add_u32 s79, s78, 1024\n\t"
    "buffer_load_dwordx4 v2, %[rw], s79 offen sc1 lds\n\t"
    "s_add_u32 s68, s68, 262144\n\t"
    "s_add_u32 s78, s78, 262144\n\t"
    "s_cmp_lt_u32 s63, 63\n\t"
    "s_cselect_b32 s61, 2048, 0\n\t"
    "s_add_u32 s73, s73, s61\n\t"
    "buffer_load_dwordx2 v[76:77], off, %[rw], s73 sc1\n\t"
    "s_nop 7\n\t"
    "s_branch LTOP_%=\n\t"
    "LLAST_%=:\n\t"
    "s_nop 7\n\t"
    "s_waitcnt lgkmcnt(0)\n\t"
    "s_lshl_b32 s61, %[w], 8\n\t"
    "v_add_u32 v40, s61, v4\n\t"
    "ds_read_b64_tr_b16 v[64:65], v40 offset:4608\n\t"
    "ds_read_b64_tr_b16 v[66:67], v40 offset:4736\n\t"
    "s_waitcnt lgkmcnt(0)\n\t"
    "buffer_store_dwordx4 v[64:67], v0, %[rw], s69 offen offset:0 sc1\n\t"
    "s_waitcnt vmcnt(0)\n\t"
    "buffer_store_dword v1, off, %[rw], s72 sc1\n\t"
    "s_branch LOUT_%=\n\t"
    "LEND_%=:\n\t"
    "v_mov_b32 v12, 1\n\t"
    "v_mov_b32 v13, 0\n\t"
    "s_mov_b32 s61, 2097216\n\t"
    "s_nop 1\n\t"
    "buffer_store_dword v12, v13, %[rw], s61 offen sc1\n\t"
    "LOUT_%=:\n\t"
    "s_waitcnt vmcnt(0) lgkmcnt(0)\n\t"
    "s_endpgm\n\t"
    :: [rq] "s"(rq), [rk] "s"(rk), [ro] "s"(ro), [rw] "s"(rw), [h] "s"(h), [w] "s"(w), [slot] "s"(slot)
    : "memory", "vcc", "scc", "v0", "v1", "v2", "v3", "v4", "v5", "v6", "v7", "v8", "v9", "v10", "v11", "v12", "v13", "v14", "v15", "v16", "v17", "v18", "v19", "v20", "v21", "v22", "v23", "v24", "v25", "v26", "v27", "v28", "v29", "v30", "v31", "v32", "v33", "v34", "v35", "v36", "v37", "v38", "v39", "v40", "v41", "v42", "v43", "v44", "v45", "v46", "v47", "v48", "v49", "v50", "v51", "v52", "v53", "v54", "v55", "v56", "v57", "v58", "v59", "v60", "v61", "v62", "v63", "v64", "v65", "v66", "v67", "v68", "v69", "v70", "v71", "v72", "v73", "v74", "v75", "v76", "v77", "v78", "v79", "v80", "v81", "v82", "v83", "v84", "v85", "v86", "v87", "v88", "v89", "v90", "v91", "v92", "v93", "v94", "v95", "v96", "v97", "v98", "v99", "v100", "v101", "v102", "v103", "v104", "v105", "v106", "v107", "v108", "v109", "v110", "v111", "v112", "v113", "v114", "v115", "v116", "v117", "v118", "v119", "v120", "v121", "v122", "v123", "v124", "v125", "v126", "v127", "v128", "v129", "v130", "v131", "v132", "v133", "v134", "v135", "v136", "v137", "v138", "v139", "v140", "v141", "v142", "v143", "v144", "v145", "v146", "v147", "v148", "v149", "v150", "v151", "v152", "v153", "v154", "v155", "v156", "v157", "v158", "v159", "v160", "v161", "v162", "v163", "v164", "v165", "v166", "v167", "v168", "v169", "v170", "v171", "v172", "v173", "v174", "v175", "v176", "v177", "v178", "v179", "v180", "v181", "v182", "v183", "v184", "v185", "v186", "v187", "v188", "v189", "v190", "v191", "v192", "v193", "v194", "v195", "v196", "v197", "v198", "v199", "v200", "v201", "v202", "v203", "v204", "v205", "v206", "v207", "v208", "v209", "v210", "v211", "v212", "v213", "v214", "v215", "v216", "v217", "v218", "v219", "v220", "v221", "v222", "v223", "v224", "v225", "v226", "v227", "v228", "v229", "v230", "v231", "v232", "v233", "v234", "v235", "v236", "v237", "v238", "v239", "v240", "v241", "v242", "v243", "v244", "v245", "v246", "v247", "v248", "v249", "v250", "v251", "v252", "v253", "v254", "v255", "a0", "a1", "a2", "a3", "a4", "a5", "a6", "a7", "a8", "a9", "a10", "a11", "a12", "a13", "a14", "a15", "a16", "a17", "a18", "a19", "a20", "a21", "a22", "a23", "a24", "a25", "a26", "a27", "a28", "a29", "a30", "a31", "a32", "a33", "a34", "a35", "a36", "a37", "a38", "a39", "a40", "a41", "a42", "a43", "a44", "a45", "a46", "a47", "a48", "a49", "a50", "a51", "a52", "a53", "a54", "a55", "a56", "a57", "a58", "a59", "a60", "a61", "a62", "a63", "a64", "a65", "a66", "a67", "a68", "a69", "a70", "a71", "a72", "a73", "a74", "a75", "a76", "a77", "a78", "a79", "a80", "a81", "a82", "a83", "a84", "a85", "a86", "a87", "a88", "a89", "a90", "a91", "a92", "a93", "a94", "a95", "a96", "a97", "a98", "a99", "a100", "a101", "a102", "a103", "a104", "a105", "a106", "a107", "a108", "a109", "a110", "a111", "a112", "a113", "a114", "a115", "a116", "a117", "a118", "a119", "a120", "a121", "a122", "a123", "a124", "a125", "a126", "a127", "a128", "a129", "a130", "a131", "a132", "a133", "a134", "a135", "a136", "a137", "a138", "a139", "a140", "a141", "a142", "a143", "a144", "a145", "a146", "a147", "a148", "a149", "a150", "a151", "a152", "a153", "a154", "a155", "a156", "a157", "a158", "a159", "a160", "a161", "a162", "a163", "a164", "a165", "a166", "a167", "a168", "a169", "a170", "a171", "a172", "a173", "a174", "a175", "a176", "a177", "a178", "a179", "a180", "a181", "a182", "a183", "a184", "a185", "a186", "a187", "a188", "a189", "a190", "a191", "a192", "a193", "a194", "a195", "a196", "a197", "a198", "a199", "a200", "a201", "a202", "a203", "a204", "a205", "a206", "a207", "a208", "a209", "a210", "a211", "a212", "a213", "a214", "a215", "a216", "a217", "a218", "a219", "a220", "a221", "a222", "a223", "a224", "a225", "a226", "a227", "a228", "a229", "a230", "a231", "a232", "a233", "a234", "a235", "a236", "a237", "a238", "a239", "a240", "a241", "a242", "a243", "a244", "a245", "a246", "a247", "a248", "a249", "a250", "a251", "a252", "a253", "a254", "a255", "s60", "s61", "s62", "s63", "s64", "s65", "s66", "s67", "s68", "s69", "s70", "s71", "s72", "s73", "s74", "s75", "s76", "s77", "s78", "s79", "s80", "s81", "s82", "s83", "s84", "s85", "s86", "s87", "s88", "s89", "s90", "s91", "s92", "s93", "s94", "s95", "s96", "s97", "s98", "s99", "s100", "s101");
}
__device__ __forceinline__ void vcu_asm(rsrc_t rv, rsrc_t rw, int h, int w, int slot) {
    asm volatile(
    "s_lshl_b32 s98, %[slot], 2\n\t"
    "s_add_u32 s98, s98, 3149824\n\t"
    "buffer_load_dword v56, off, %[rw], s98 sc1\n\t"
    "s_add_u32 s98, s98, 4096\n\t"
    "buffer_load_dword v57, off, %[rw], s98 sc1\n\t"
    "s_sub_u32 s98, s98, 4096\n\t"
    "v_mbcnt_lo_u32_b32 v60, -1, 0\n\t"
    "v_mbcnt_hi_u32_b32 v60, -1, v60\n\t"
    "s_nop 1\n\t"
    "s_lshl_b32 s60, %[w], 15\n\t"
    "s_lshl_b32 s69, %[w], 11\n\t"
    "v_lshrrev_b32 v12, 2, v60\n\t"
    "v_lshlrev_b32 v61, 6, v12\n\t"
    "v_bfe_u32 v12, v60, 4, 1\n\t"
    "v_lshl_add_u32 v61, v12, 5, v61\n\t"
    "v_and_b32 v12, 3, v60\n\t"
    "v_lshl_add_u32 v61, v12, 3, v61\n\t"
    "v_add_u32 v61, s60, v61\n\t"
    "v_xor_b32 v62, 32, v61\n\t"
    "v_bfe_u32 v20, v60, 4, 1\n\t"
    "v_lshrrev_b32 v21, 5, v60\n\t"
    "v_bfe_u32 v22, v60, 2, 1\n\t"
    "v_bfe_u32 v23, v60, 3, 1\n\t"
    "v_bfe_u32 v12, v60, 1, 1\n\t"
    "v_xor_b32 v12, v12, v20\n\t"
    "v_lshlrev_b32 v19, 5, v12\n\t"
    "v_and_b32 v12, 1, v60\n\t"
    "v_lshl_add_u32 v19, v12, 4, v19\n\t"
    "v_lshlrev_b32 v12, 6, v20\n\t"
    "v_lshl_add_u32 v12, v22, 4, v12\n\t"
    "v_lshl_add_u32 v12, v21, 1, v12\n\t"
    "v_add_u32 v12, v12, v23\n\t"
    "v_lshl_add_u32 v16, v12, 13, v19\n\t"
    "v_mul_u32_u24 v12, 144, v21\n\t"
    "v_lshl_add_u32 v12, v23, 3, v12\n\t"
    "v_lshl_add_u32 v12, v20, 2, v12\n\t"
    "v_add_u32 v12, v12, v22\n\t"
    "v_lshl_add_u32 v17, v12, 13, v19\n\t"
    "v_lshlrev_b32 v12, 4, v23\n\t"
    "v_lshl_add_u32 v12, v20, 2, v12\n\t"
    "v_lshl_add_u32 v12, v21, 1, v12\n\t"
    "v_add_u32 v12, v12, v22\n\t"
    "v_mul_u32_u24 v18, 1152, v12\n\t"
    "v_and_b32 v12, 3, v60\n\t"
    "v_lshl_add_u32 v18, v12, 4, v18\n\t"
    "s_lshl_b32 s72, %[h], 8\n\t"
    "s_lshl_b32 s61, %[w], 6\n\t"
    "s_add_u32 s72, s72, s61\n\t"
    "s_mul_i32 s73, %[h], 147456\n\t"
    "s_mul_i32 s61, %[w], 288\n\t"
    "s_add_u32 s73, s73, s61\n\t"
    "s_xor_b32 s61, %[w], 0\n\t"
    "s_mul_i32 s61, s61, 1179648\n\t"
    "s_add_u32 s80, s61, s72\n\t"
    "s_xor_b32 s61, %[w], 1\n\t"
    "s_mul_i32 s61, s61, 1179648\n\t"
    "s_add_u32 s81, s61, s72\n\t"
    "s_xor_b32 s61, %[w], 2\n\t"
    "s_mul_i32 s61, s61, 1179648\n\t"
    "s_add_u32 s82, s61, s72\n\t"
    "s_xor_b32 s61, %[w], 3\n\t"
    "s_mul_i32 s61, s61, 1179648\n\t"
    "s_add_u32 s83, s61, s72\n\t"
    "s_add_u32 s61, s80, 0\n\t"
    "s_add_u32 m0, s60, 0\n\t"
    "s_nop 0\n\t"
    "buffer_load_dwordx4 v16, %[rv], s61 offen lds\n\t"
    "s_add_u32 s61, s80, 262144\n\t"
    "s_add_u32 m0, s60, 1024\n\t"
    "s_nop 0\n\t"
    "buffer_load_dwordx4 v16, %[rv], s61 offen lds\n\t"
    "s_add_u32 s61, s80, 32768\n\t"
    "s_add_u32 m0, s60, 2048\n\t"
    "s_nop 0\n\t"
    "buffer_load_dwordx4 v16, %[rv], s61 offen lds\n\t"
    "s_add_u32 s61, s80, 294912\n\t"
    "s_add_u32 m0, s60, 3072\n\t"
    "s_nop 0\n\t"
    "buffer_load_dwordx4 v16, %[rv], s61 offen lds\n\t"
    "s_add_u32 s61, s80, 65536\n\t"
    "s_add_u32 m0, s60, 4096\n\t"
    "s_nop 0\n\t"
    "buffer_load_dwordx4 v16, %[rv], s61 offen lds\n\t"
    "s_add_u32 s61, s80, 327680\n\t"
    "s_add_u32 m0, s60, 5120\n\t"
    "s_nop 0\n\t"
    "buffer_load_dwordx4 v16, %[rv], s61 offen lds\n\t"
    "s_add_u32 s61, s80, 98304\n\t"
    "s_add_u32 m0, s60, 6144\n\t"
    "s_nop 0\n\t"
    "buffer_load_dwordx4 v16, %[rv], s61 offen lds\n\t"
    "s_add_u32 s61, s80, 360448\n\t"
    "s_add_u32 m0, s60, 7168\n\t"
    "s_nop 0\n\t"
    "buffer_load_dwordx4 v16, %[rv], s61 offen lds\n\t"
    "s_add_u32 s61, s81, 0\n\t"
    "s_add_u32 m0, s60, 8192\n\t"
    "s_nop 0\n\t"
    "buffer_load_dwordx4 v16, %[rv], s61 offen lds\n\t"
    "s_add_u32 s61, s81, 262144\n\t"
    "s_add_u32 m0, s60, 9216\n\t"
    "s_nop 0\n\t"
    "buffer_load_dwordx4 v16, %[rv], s61 offen lds\n\t"
    "s_add_u32 s61, s81, 32768\n\t"
    "s_add_u32 m0, s60, 10240\n\t"
    "s_nop 0\n\t"
    "buffer_load_dwordx4 v16, %[rv], s61 offen lds\n\t"
    "s_add_u32 s61, s81, 294912\n\t"
    "s_add_u32 m0, s60, 11264\n\t"
    "s_nop 0\n\t"
    "buffer_load_dwordx4 v16, %[rv], s61 offen lds\n\t"
    "s_add_u32 s61, s81, 65536\n\t"
    "s_add_u32 m0, s60, 12288\n\t"
    "s_nop 0\n\t"
    "buffer_load_dwordx4 v16, %[rv], s61 offen lds\n\t"
    "s_add_u32 s61, s81, 327680\n\t"
    "s_add_u32 m0, s60, 13312\n\t"
    "s_nop 0\n\t"
    "buffer_load_dwordx4 v16, %[rv], s61 offen lds\n\t"
    "s_add_u32 s61, s81, 98304\n\t"
    "s_add_u32 m0, s60, 14336\n\t"
    "s_nop 0\n\t"
    "buffer_load_dwordx4 v16, %[rv], s61 offen lds\n\t"
    "s_add_u32 s61, s81, 360448\n\t"
    "s_add_u32 m0, s60, 15360\n\t"
    "s_nop 0\n\t"
    "buffer_load_dwordx4 v16, %[rv], s61 offen lds\n\t"
    "s_add_u32 s61, s82, 0\n\t"
    "s_add_u32 m0, s60, 16384\n\t"
    "s_nop 0\n\t"
    "buffer_load_dwordx4 v16, %[rv], s61 offen lds\n\t"
    "s_add_u32 s61, s82, 262144\n\t"
    "s_add_u32 m0, s60, 17408\n\t"
    "s_nop 0\n\t"
    "buffer_load_dwordx4 v16, %[rv], s61 offen lds\n\t"
    "s_add_u32 s61, s82, 32768\n\t"
    "s_add_u32 m0, s60, 18432\n\t"
    "s_nop 0\n\t"
    "buffer_load_dwordx4 v16, %[rv], s61 offen lds\n\t"
    "s_add_u32 s61, s82, 294912\n\t"
    "s_add_u32 m0, s60, 19456\n\t"
    "s_nop 0\n\t"
    "buffer_load_dwordx4 v16, %[rv], s61 offen lds\n\t"
    "s_add_u32 s61, s82, 65536\n\t"
    "s_add_u32 m0, s60, 20480\n\t"
    "s_nop 0\n\t"
    "buffer_load_dwordx4 v16, %[rv], s61 offen lds\n\t"
    "s_add_u32 s61, s82, 327680\n\t"
    "s_add_u32 m0, s60, 21504\n\t"
    "s_nop 0\n\t"
    "buffer_load_dwordx4 v16, %[rv], s61 offen lds\n\t"
    "s_add_u32 s61, s82, 98304\n\t"
    "s_add_u32 m0, s60, 22528\n\t"
    "s_nop 0\n\t"
    "buffer_load_dwordx4 v16, %[rv], s61 offen lds\n\t"
    "s_add_u32 s61, s82, 360448\n\t"
    "s_add_u32 m0, s60, 23552\n\t"
    "s_nop 0\n\t"
    "buffer_load_dwordx4 v16, %[rv], s61 offen lds\n\t"
    "s_add_u32 s61, s83, 0\n\t"
    "s_add_u32 m0, s60, 24576\n\t"
    "s_nop 0\n\t"
    "buffer_load_dwordx4 v16, %[rv], s61 offen lds\n\t"
    "s_add_u32 s61, s83, 262144\n\t"
    "s_add_u32 m0, s60, 25600\n\t"
    "s_nop 0\n\t"
    "buffer_load_dwordx4 v16, %[rv], s61 offen lds\n\t"
    "s_add_u32 s61, s83, 32768\n\t"
    "s_add_u32 m0, s60, 26624\n\t"
    "s_nop 0\n\t"
    "buffer_load_dwordx4 v16, %[rv], s61 offen lds\n\t"
    "s_add_u32 s61, s83, 294912\n\t"
    "s_add_u32 m0, s60, 27648\n\t"
    "s_nop 0\n\t"
    "buffer_load_dwordx4 v16, %[rv], s61 offen lds\n\t"
    "s_add_u32 s61, s83, 65536\n\t"
    "s_add_u32 m0, s60, 28672\n\t"
    "s_nop 0\n\t"
    "buffer_load_dwordx4 v16, %[rv], s61 offen lds\n\t"
    "s_add_u32 s61, s83, 327680\n\t"
    "s_add_u32 m0, s60, 29696\n\t"
    "s_nop 0\n\t"
    "buffer_load_dwordx4 v16, %[rv], s61 offen lds\n\t"
    "s_add_u32 s61, s83, 98304\n\t"
    "s_add_u32 m0, s60, 30720\n\t"
    "s_nop 0\n\t"
    "buffer_load_dwordx4 v16, %[rv], s61 offen lds\n\t"
    "s_add_u32 s61, s83, 360448\n\t"
    "s_add_u32 m0, s60, 31744\n\t"
    "s_nop 0\n\t"
    "buffer_load_dwordx4 v16, %[rv], s61 offen lds\n\t"
    "v_lshlrev_b32 v4, 4, v60\n\t"
    "v_add_u32 v0, 131072, v4\n\t"
    "v_add_u32 v2, s69, v0\n\t"
    "v_bfe_u32 v12, v60, 1, 1\n\t"
    "v_mul_u32_u24 v12, 43690, v12\n\t"
    "v_add_u32 v1, 17476, v12\n\t"
    "v_and_b32 v3, 240, v0\n\t"
    "s_lshl_b32 s61, %[w], 8\n\t"
    "s_add_u32 s61, s61, 139264\n\t"
    "v_add_u32 v3, s61, v3\n\t"
    "v_xor_b32 v5, 2048, v2\n\t"
    "v_xor_b32 v6, 4096, v2\n\t"
    "v_xor_b32 v7, 6144, v2\n\t"
    "s_waitcnt vmcnt(33)\n\t"
    "v_add_u32 v58, 1, v56\n\t"
    "v_add_u32 v59, 2, v56\n\t"
    "s_waitcnt vmcnt(32)\n\t"
    "v_cmp_eq_u32 vcc, v58, v57\n\t"
    "s_nop 1\n\t"
    "v_cndmask_b32 v58, v58, v59, vcc\n\t"
    "s_nop 0\n\t"
    "v_readfirstlane_b32 s99, v58\n\t"
    "buffer_store_dword v58, off, %[rw], s98 sc1\n\t"
    "s_add_u32 s98, s98, 4096\n\t"
    "s_waitcnt vmcnt(29)\n\t"
    "ds_read_b64_tr_b16 v[64:65], v61 offset:0\n\t"
    "ds_read_b64_tr_b16 v[66:67], v61 offset:1024\n\t"
    "ds_read_b64_tr_b16 v[68:69], v61 offset:2048\n\t"
    "ds_read_b64_tr_b16 v[70:71], v61 offset:3072\n\t"
    "ds_read_b64_tr_b16 v[72:73], v62 offset:0\n\t"
    "ds_read_b64_tr_b16 v[74:75], v62 offset:1024\n\t"
    "ds_read_b64_tr_b16 v[76:77], v62 offset:2048\n\t"
    "ds_read_b64_tr_b16 v[78:79], v62 offset:3072\n\t"
    "s_waitcnt vmcnt(25)\n\t"
    "ds_read_b64_tr_b16 v[80:81], v61 offset:4096\n\t"
    "ds_read_b64_tr_b16 v[82:83], v61 offset:5120\n\t"
    "ds_read_b64_tr_b16 v[84:85], v61 offset:6144\n\t"
    "ds_read_b64_tr_b16 v[86:87], v61 offset:7168\n\t"
    "ds_read_b64_tr_b16 v[88:89], v62 offset:4096\n\t"
    "ds_read_b64_tr_b16 v[90:91], v62 offset:5120\n\t"
    "ds_read_b64_tr_b16 v[92:93], v62 offset:6144\n\t"
    "s_waitcnt lgkmcnt(14)\n\t"
    "ds_read_b64_tr_b16 v[94:95], v62 offset:7168\n\t"
    "s_waitcnt lgkmcnt(8)\n\t"
    "s_add_u32 s61, s72, 1048576\n\t"
    "s_add_u32 m0, s60, 0\n\t"
    "s_nop 0\n\t"
    "buffer_load_dwordx4 v17, %[rv], s61 offen lds\n\t"
    "s_add_u32 s61, s72, 1064960\n\t"
    "s_add_u32 m0, s60, 1024\n\t"
    "s_nop 0\n\t"
    "buffer_load_dwordx4 v17, %[rv], s61 offen lds\n\t"
    "s_add_u32 s61, s72, 3407872\n\t"
    "s_add_u32 m0, s60, 2048\n\t"
    "s_nop 0\n\t"
    "buffer_load_dwordx4 v17, %[rv], s61 offen lds\n\t"
    "s_add_u32 s61, s72, 3424256\n\t"
    "s_add_u32 m0, s60, 3072\n\t"
    "s_nop 0\n\t"
    "buffer_load_dwordx4 v17, %[rv], s61 offen lds\n\t"
    "s_waitcnt vmcnt(25)\n\t"
    "ds_read_b64_tr_b16 v[96:97], v61 offset:8192\n\t"
    "ds_read_b64_tr_b16 v[98:99], v61 offset:9216\n\t"
    "ds_read_b64_tr_b16 v[100:101], v61 offset:10240\n\t"
    "ds_read_b64_tr_b16 v[102:103], v61 offset:11264\n\t"
    "ds_read_b64_tr_b16 v[104:105], v62 offset:8192\n\t"
    "ds_read_b64_tr_b16 v[106:107], v62 offset:9216\n\t"
    "ds_read_b64_tr_b16 v[108:109], v62 offset:10240\n\t"
    "s_waitcnt lgkmcnt(14)\n\t"
    "ds_read_b64_tr_b16 v[110:111], v62 offset:11264\n\t"
    "s_waitcnt vmcnt(21)\n\t"
    "s_waitcnt lgkmcnt(14)\n\t"
    "ds_read_b64_tr_b16 v[112:113], v61 offset:12288\n\t"
    "s_waitcnt lgkmcnt(14)\n\t"
    "ds_read_b64_tr_b16 v[114:115], v61 offset:13312\n\t"
    "s_waitcnt lgkmcnt(14)\n\t"
    "ds_read_b64_tr_b16 v[116:117], v61 offset:14336\n\t"
    "s_waitcnt lgkmcnt(14)\n\t"
    "ds_read_b64_tr_b16 v[118:119], v61 offset:15360\n\t"
    "s_waitcnt lgkmcnt(14)\n\t"
    "ds_read_b64_tr_b16 v[120:121], v62 offset:12288\n\t"
    "s_waitcnt lgkmcnt(14)\n\t"
    "ds_read_b64_tr_b16 v[122:123], v62 offset:13312\n\t"
    "s_waitcnt lgkmcnt(14)\n\t"
    "ds_read_b64_tr_b16 v[124:125], v62 offset:14336\n\t"
    "s_waitcnt lgkmcnt(14)\n\t"
    "ds_read_b64_tr_b16 v[126:127], v62 offset:15360\n\t"
    "s_waitcnt vmcnt(17)\n\t"
    "s_waitcnt lgkmcnt(14)\n\t"
    "ds_read_b64_tr_b16 v[128:129], v61 offset:16384\n\t"
    "s_waitcnt lgkmcnt(14)\n\t"
    "ds_read_b64_tr_b16 v[130:131], v61 offset:17408\n\t"
    "s_waitcnt lgkmcnt(14)\n\t"
    "ds_read_b64_tr_b16 v[132:133], v61 offset:18432\n\t"
    "s_waitcnt lgkmcnt(14)\n\t"
    "ds_read_b64_tr_b16 v[134:135], v61 offset:19456\n\t"
    "s_waitcnt lgkmcnt(14)\n\t"
    "ds_read_b64_tr_b16 v[136:137], v62 offset:16384\n\t"
    "s_waitcnt lgkmcnt(14)\n\t"
    "ds_read_b64_tr_b16 v[138:139], v62 offset:17408\n\t"
    "s_waitcnt lgkmcnt(14)\n\t"
    "ds_read_b64_tr_b16 v[140:141], v62 offset:18432\n\t"
    "s_waitcnt lgkmcnt(14)\n\t"
    "ds_read_b64_tr_b16 v[142:143], v62 offset:19456\n\t"
    "s_waitcnt vmcnt(13)\n\t"
    "s_waitcnt lgkmcnt(14)\n\t"
    "ds_read_b64_tr_b16 v[144:145], v61 offset:20480\n\t"
    "s_waitcnt lgkmcnt(14)\n\t"
    "ds_read_b64_tr_b16 v[146:147], v61 offset:21504\n\t"
    "s_waitcnt lgkmcnt(14)\n\t"
    "ds_read_b64_tr_b16 v[148:149], v61 offset:22528\n\t"
    "s_waitcnt lgkmcnt(14)\n\t"
    "ds_read_b64_tr_b16 v[150:151], v61 offset:23552\n\t"
    "s_waitcnt lgkmcnt(14)\n\t"
    "ds_read_b64_tr_b16 v[152:153], v62 offset:20480\n\t"
    "s_waitcnt lgkmcnt(14)\n\t"
    "ds_read_b64_tr_b16 v[154:155], v62 offset:21504\n\t"
    "s_waitcnt lgkmcnt(14)\n\t"
    "ds_read_b64_tr_b16 v[156:157], v62 offset:22528\n\t"
    "s_waitcnt lgkmcnt(14)\n\t"
    "ds_read_b64_tr_b16 v[158:159], v62 offset:23552\n\t"
    "s_waitcnt vmcnt(9)\n\t"
    "s_waitcnt lgkmcnt(14)\n\t"
    "ds_read_b64_tr_b16 v[160:161], v61 offset:24576\n\t"
    "s_waitcnt lgkmcnt(14)\n\t"
    "ds_read_b64_tr_b16 v[162:163], v61 offset:25600\n\t"
    "s_waitcnt lgkmcnt(14)\n\t"
    "ds_read_b64_tr_b16 v[164:165], v61 offset:26624\n\t"
    "s_waitcnt lgkmcnt(14)\n\t"
    "ds_read_b64_tr_b16 v[166:167], v61 offset:27648\n\t"
    "s_waitcnt lgkmcnt(14)\n\t"
    "ds_read_b64_tr_b16 v[168:169], v62 offset:24576\n\t"
    "s_waitcnt lgkmcnt(14)\n\t"
    "ds_read_b64_tr_b16 v[170:171], v62 offset:25600\n\t"
    "s_waitcnt lgkmcnt(14)\n\t"
    "ds_read_b64_tr_b16 v[172:173], v62 offset:26624\n\t"
    "s_waitcnt lgkmcnt(14)\n\t"
    "ds_read_b64_tr_b16 v[174:175], v62 offset:27648\n\t"
    "s_waitcnt vmcnt(5)\n\t"
    "s_waitcnt lgkmcnt(14)\n\t"
    "ds_read_b64_tr_b16 v[176:177], v61 offset:28672\n\t"
    "s_waitcnt lgkmcnt(14)\n\t"
    "ds_read_b64_tr_b16 v[178:179], v61 offset:29696\n\t"
    "s_waitcnt lgkmcnt(14)\n\t"
    "ds_read_b64_tr_b16 v[180:181], v61 offset:30720\n\t"
    "s_waitcnt lgkmcnt(14)\n\t"
    "ds_read_b64_tr_b16 v[182:183], v61 offset:31744\n\t"
    "s_waitcnt lgkmcnt(14)\n\t"
    "ds_read_b64_tr_b16 v[184:185], v62 offset:28672\n\t"
    "s_waitcnt lgkmcnt(14)\n\t"
    "ds_read_b64_tr_b16 v[186:187], v62 offset:29696\n\t"
    "s_waitcnt lgkmcnt(14)\n\t"
    "ds_read_b64_tr_b16 v[188:189], v62 offset:30720\n\t"
    "s_waitcnt lgkmcnt(14)\n\t"
    "ds_read_b64_tr_b16 v[190:191], v62 offset:31744\n\t"
    "s_waitcnt vmcnt(0)\n\t"
    "s_waitcnt lgkmcnt(14)\n\t"
    "ds_read_b64_tr_b16 v[192:193], v61 offset:0\n\t"
    "s_waitcnt lgkmcnt(14)\n\t"
    "ds_read_b64_tr_b16 v[194:195], v61 offset:1024\n\t"
    "s_waitcnt lgkmcnt(14)\n\t"
    "ds_read_b64_tr_b16 v[196:197], v61 offset:2048\n\t"
    "s_waitcnt lgkmcnt(14)\n\t"
    "ds_read_b64_tr_b16 v[198:199], v61 offset:3072\n\t"
    "s_waitcnt lgkmcnt(14)\n\t"
    "ds_read_b64_tr_b16 v[200:201], v62 offset:0\n\t"
    "s_waitcnt lgkmcnt(14)\n\t"
    "ds_read_b64_tr_b16 v[202:203], v62 offset:1024\n\t"
    "s_waitcnt lgkmcnt(14)\n\t"
    "ds_read_b64_tr_b16 v[204:205], v62 offset:2048\n\t"
    "s_waitcnt lgkmcnt(14)\n\t"
    "ds_read_b64_tr_b16 v[206:207], v62 offset:3072\n\t"
    "s_waitcnt lgkmcnt(0)\n\t"
    "s_mov_b32 s63, 0\n\t"
    "s_mov_b32 s67, 0x200000\n\t"
    "LHS_%=:\n\t"
    "buffer_load_dword v56, off, %[rw], s98 sc1\n\t"
    "s_waitcnt vmcnt(0)\n\t"
    "v_readfirstlane_b32 s95, v56\n\t"
    "s_cmp_eq_u32 s95, s99\n\t"
    "s_cbranch_scc1 LHSD_%=\n\t"
    "s_sleep 2\n\t"
    "s_sub_u32 s67, s67, 1\n\t"
    "s_cmp_lg_u32 s67, 0\n\t"
    "s_cbranch_scc1 LHS_%=\n\t"
    "s_branch LOUT_%=\n\t"
    "LHSD_%=:\n\t"
    "s_mov_b32 s98, 2097536\n\t"
    "LSE_%=:\n\t"
    "buffer_load_dword v56, off, %[rw], s98 sc1\n\t"
    "s_waitcnt vmcnt(0)\n\t"
    "v_readfirstlane_b32 s95, v56\n\t"
    "s_cmp_lg_u32 s95, 0\n\t"
    "s_cbranch_scc1 LSED_%=\n\t"
    "s_sleep 2\n\t"
    "s_sub_u32 s67, s67, 1\n\t"
    "s_cmp_lg_u32 s67, 0\n\t"
    "s_cbranch_scc1 LSE_%=\n\t"
    "s_branch LEND_%=\n\t"
    "LSED_%=:\n\t"
    "s_sub_u32 s95, s95, 1\n\t"
    "s_lshl_b32 s95, s95, 13\n\t"
    "s_lshl_b32 s98, %[slot], 2\n\t"
    "s_add_u32 s98, s98, 2113536\n\t"
    "v_mov_b32 v56, 1\n\t"
    "buffer_store_dword v56, off, %[rw], s98 sc1\n\t"
    "s_lshl_b32 s66, %[h], 12\n\t"
    "s_lshl_b32 s61, %[w], 10\n\t"
    "s_add_u32 s66, s66, s61\n\t"
    "s_add_u32 s66, s66, 46137344\n\t"
    "s_lshr_b32 s75, %[h], 1\n\t"
    "s_lshl_b32 s75, s75, 14\n\t"
    "s_and_b32 s62, %[h], 1\n\t"
    "s_lshl_b32 s62, s62, 12\n\t"
    "s_add_u32 s75, s75, s62\n\t"
    "s_add_u32 s75, s75, s61\n\t"
    "s_add_u32 s75, s75, s95\n\t"
    "s_add_u32 s75, s75, 1572864\n\t"
    "s_mov_b32 s76, 0\n\t"
    "s_lshl_b32 s68, %[h], 6\n\t"
    "s_add_u32 s68, s68, 2162704\n\t"
    "s_lshl_b32 s61, %[w], 2\n\t"
    "s_add_u32 s68, s68, s61\n\t"
    "s_and_b32 s61, %[h], 3\n\t"
    "s_lshl_b32 s61, s61, 15\n\t"
    "s_add_u32 s77, s61, s95\n\t"
    "s_add_u32 s77, s77, 1048576\n\t"
    "s_mov_b32 s70, 0\n\t"
    "s_mov_b32 s84, 0\n\t"
    "s_nop 7\n\t"
    "LTOP_%=:\n\t"
    "v_mov_b64 v[24:25], 0\n\t"
    "v_mov_b64 v[26:27], 0\n\t"
    "v_mov_b64 v[28:29], 0\n\t"
    "v_mov_b64 v[30:31], 0\n\t"
    "s_add_u32 s64, s77, s70\n\t"
    "s_sub_u32 s64, s64, s84\n\t"
    "s_add_u32 s65, s64, 8192\n\t"
    "s_add_u32 s74, s75, s76\n\t"
    "s_waitcnt vmcnt(0)\n\t"
    "s_mov_b32 exec_lo, 0x00010001\n\t"
    "s_mov_b32 exec_hi, 0x00010001\n\t"
    "LP_%=:\n\t"
    "s_nop 1\n\t"
    "buffer_load_dword v60, v2, %[rw], s64 offen offset:12 sc1\n\t"
    "s_waitcnt vmcnt(0)\n\t"
    "v_cmp_ne_u32 vcc, -1, v60\n\t"
    "s_cbranch_vccnz LPD_%=\n\t"
    "s_sleep 1\n\t"
    "s_sub_u32 s67, s67, 1\n\t"
    "s_cmp_lg_u32 s67, 0\n\t"
    "s_cbranch_scc1 LP_%=\n\t"
    "LPD_%=:\n\t"
    "s_mov_b64 exec, -1\n\t"
    "LG_%=:\n\t"
    "s_nop 1\n\t"
    "buffer_load_dwordx4 v[12:15], v2, %[rw], s64 offen offset:0 sc1\n\t"
    "buffer_load_dwordx4 v[16:19], v2, %[rw], s64 offen offset:1024 sc1\n\t"
    "buffer_load_dwordx4 v[20:23], v3, %[rw], s65 offen offset:0 sc1\n\t"
    "s_waitcnt vmcnt(2)\n\t"
    "v_max3_u32 v60, v12, v13, v14\n\t"
    "s_waitcnt vmcnt(1)\n\t"
    "v_max3_u32 v61, v15, v16, v17\n\t"
    "v_max3_u32 v60, v60, v18, v19\n\t"
    "s_waitcnt vmcnt(0)\n\t"
    "v_max3_u32 v61, v61, v20, v21\n\t"
    "v_max3_u32 v60, v60, v22, v23\n\t"
    "v_max_u32 v60, v60, v61\n\t"
    "v_cmp_eq_u32 vcc, -1, v60\n\t"
    "s_cbranch_vccz LGOK_%=\n\t"
    "s_sub_u32 s67, s67, 1\n\t"
    "s_cmp_lg_u32 s67, 0\n\t"
    "s_cbranch_scc1 LG_%=\n\t"
    "s_branch LEND_%=\n\t"
    "LGOK_%=:\n\t"
    "ds_write_b128 v2, v[12:15] offset:0\n\t"
    "ds_write_b128 v2, v[16:19] offset:1024\n\t"
    "ds_write_b128 v3, v[20:23] offset:0\n\t"
    "v_smfmac_f32_16x16x64_bf16 v[24:27], v[12:15], v[64:71], v1\n\t"
    "v_smfmac_f32_16x16x64_bf16 v[28:31], v[12:15], v[72:79], v1\n\t"
    "v_smfmac_f32_16x16x64_bf16 v[24:27], v[16:19], v[80:87], v1\n\t"
    "s_waitcnt lgkmcnt(0)\n\t"
    "s_barrier\n\t"
    "ds_read_b128 v[20:23], v5 offset:0\n\t"
    "ds_read_b128 v[32:35], v5 offset:1024\n\t"
    "ds_read_b128 v[36:39], v6 offset:0\n\t"
    "ds_read_b128 v[40:43], v6 offset:1024\n\t"
    "ds_read_b128 v[44:47], v7 offset:0\n\t"
    "ds_read_b128 v[48:51], v7 offset:1024\n\t"
    "ds_read_b128 v[52:55], v0 offset:8192\n\t"
    "v_smfmac_f32_16x16x64_bf16 v[28:31], v[16:19], v[88:95], v1\n\t"
    "s_waitcnt lgkmcnt(6)\n\t"
    "v_smfmac_f32_16x16x64_bf16 v[24:27], v[20:23], v[96:103], v1\n\t"
    "v_smfmac_f32_16x16x64_bf16 v[28:31], v[20:23], v[104:111], v1\n\t"
    "s_waitcnt lgkmcnt(5)\n\t"
    "v_smfmac_f32_16x16x64_bf16 v[24:27], v[32:35], v[112:119], v1\n\t"
    "v_smfmac_f32_16x16x64_bf16 v[28:31], v[32:35], v[120:127], v1\n\t"
    "s_waitcnt lgkmcnt(4)\n\t"
    "v_smfmac_f32_16x16x64_bf16 v[24:27], v[36:39], v[128:135], v1\n\t"
    "v_smfmac_f32_16x16x64_bf16 v[28:31], v[36:39], v[136:143], v1\n\t"
    "s_waitcnt lgkmcnt(3)\n\t"
    "v_smfmac_f32_16x16x64_bf16 v[24:27], v[40:43], v[144:151], v1\n\t"
    "v_smfmac_f32_16x16x64_bf16 v[28:31], v[40:43], v[152:159], v1\n\t"
    "s_waitcnt lgkmcnt(2)\n\t"
    "v_smfmac_f32_16x16x64_bf16 v[24:27], v[44:47], v[160:167], v1\n\t"
    "v_smfmac_f32_16x16x64_bf16 v[28:31], v[44:47], v[168:175], v1\n\t"
    "s_waitcnt lgkmcnt(1)\n\t"
    "v_smfmac_f32_16x16x64_bf16 v[24:27], v[48:51], v[176:183], v1\n\t"
    "v_smfmac_f32_16x16x64_bf16 v[28:31], v[48:51], v[184:191], v1\n\t"
    "s_waitcnt lgkmcnt(0)\n\t"
    "v_smfmac_f32_16x16x64_bf16 v[24:27], v[52:55], v[192:199], v1\n\t"
    "v_smfmac_f32_16x16x64_bf16 v[28:31], v[52:55], v[200:207], v1\n\t"
    "s_nop 4\n\t"
    "v_cvt_pk_bf16_f32 v24, v24, v25\n\t"
    "v_cvt_pk_bf16_f32 v25, v26, v27\n\t"
    "s_nop 0\n\t"
    "v_cvt_pk_bf16_f32 v26, v28, v29\n\t"
    "v_cvt_pk_bf16_f32 v27, v30, v31\n\t"
    "s_cmp_eq_u32 s63, 63\n\t"
    "s_cbranch_scc1 LLAST_%=\n\t"
    "buffer_store_dwordx4 v[24:27], v4, %[rw], s74 offen offset:0 sc1\n\t"
    "s_sleep 4\n\t"
    "s_cmp_eq_u32 s63, 0\n\t"
    "s_cbranch_scc1 LNF_%=\n\t"
    "buffer_store_dword v1, off, %[rw], s68 sc1\n\t"
    "s_add_u32 s68, s68, 2048\n\t"
    "LNF_%=:\n\t"
    "buffer_store_dwordx4 v[24:27], v4, %[rw], s66 offen offset:0 sc1\n\t"
    "s_add_u32 s66, s66, 131072\n\t"
    "s_add_u32 s63, s63, 1\n\t"
    "s_xor_b32 s76, s76, 262144\n\t"
    "s_add_u32 s70, s70, 131072\n\t"
    "s_cmp_eq_u32 s70, 393216\n\t"
    "s_cselect_b32 s70, 0, s70\n\t"
    "v_xor_b32 v0, 16384, v0\n\t"
    "v_xor_b32 v2, 16384, v2\n\t"
    "v_xor_b32 v3, 16384, v3\n\t"
    "v_xor_b32 v5, 16384, v5\n\t"
    "v_xor_b32 v6, 16384, v6\n\t"
    "v_xor_b32 v7, 16384, v7\n\t"
    "s_xor_b32 s84, s84, 16384\n\t"
    "s_sleep 30\n\t"
    "s_nop 3\n\t"
    "s_branch LTOP_%=\n\t"
    "LLAST_%=:\n\t"
    "s_nop 1\n\t"
    "buffer_store_dwordx4 v[24:27], v4, %[rw], s66 offen offset:0 sc1\n\t"
    "buffer_store_dword v1, off, %[rw], s68 sc1\n\t"
    "s_add_u32 s68, s68, 2048\n\t"
    "s_waitcnt vmcnt(0)\n\t"
    "buffer_store_dword v1, off, %[rw], s68 sc1\n\t"
    "s_branch LOUT_%=\n\t"
    "LEND_%=:\n\t"
    "v_mov_b32 v12, 1\n\t"
    "v_mov_b32 v13, 0\n\t"
    "s_mov_b32 s61, 2097216\n\t"
    "s_nop 1\n\t"
    "buffer_store_dword v12, v13, %[rw], s61 offen sc1\n\t"
    "LOUT_%=:\n\t"
    "s_waitcnt vmcnt(0) lgkmcnt(0)\n\t"
    "s_endpgm\n\t"
    :: [rv] "s"(rv), [rw] "s"(rw), [h] "s"(h), [w] "s"(w), [slot] "s"(slot)
    : "memory", "vcc", "scc", "v0", "v1", "v2", "v3", "v4", "v5", "v6", "v7", "v8", "v9", "v10", "v11", "v12", "v13", "v14", "v15", "v16", "v17", "v18", "v19", "v20", "v21", "v22", "v23", "v24", "v25", "v26", "v27", "v28", "v29", "v30", "v31", "v32", "v33", "v34", "v35", "v36", "v37", "v38", "v39", "v40", "v41", "v42", "v43", "v44", "v45", "v46", "v47", "v48", "v49", "v50", "v51", "v52", "v53", "v54", "v55", "v56", "v57", "v58", "v59", "v60", "v61", "v62", "v63", "v64", "v65", "v66", "v67", "v68", "v69", "v70", "v71", "v72", "v73", "v74", "v75", "v76", "v77", "v78", "v79", "v80", "v81", "v82", "v83", "v84", "v85", "v86", "v87", "v88", "v89", "v90", "v91", "v92", "v93", "v94", "v95", "v96", "v97", "v98", "v99", "v100", "v101", "v102", "v103", "v104", "v105", "v106", "v107", "v108", "v109", "v110", "v111", "v112", "v113", "v114", "v115", "v116", "v117", "v118", "v119", "v120", "v121", "v122", "v123", "v124", "v125", "v126", "v127", "v128", "v129", "v130", "v131", "v132", "v133", "v134", "v135", "v136", "v137", "v138", "v139", "v140", "v141", "v142", "v143", "v144", "v145", "v146", "v147", "v148", "v149", "v150", "v151", "v152", "v153", "v154", "v155", "v156", "v157", "v158", "v159", "v160", "v161", "v162", "v163", "v164", "v165", "v166", "v167", "v168", "v169", "v170", "v171", "v172", "v173", "v174", "v175", "v176", "v177", "v178", "v179", "v180", "v181", "v182", "v183", "v184", "v185", "v186", "v187", "v188", "v189", "v190", "v191", "v192", "v193", "v194", "v195", "v196", "v197", "v198", "v199", "v200", "v201", "v202", "v203", "v204", "v205", "v206", "v207", "s60", "s61", "s62", "s63", "s64", "s65", "s66", "s67", "s68", "s69", "s70", "s71", "s72", "s73", "s74", "s75", "s76", "s77", "s78", "s79", "s80", "s81", "s82", "s83", "s84", "s85", "s86", "s87", "s88", "s89", "s90", "s91", "s92", "s93", "s94", "s95", "s96", "s97", "s98", "s99", "s100", "s101");
}
__device__ __forceinline__ void red_asm(rsrc_t rw, unsigned addr, unsigned maddr, v4u sa, unsigned dbg) {
    asm volatile(
    "s_mov_b32 s60, 0\n\t"
    "s_mov_b32 s61, 131072\n\t"
    "s_mov_b32 s62, 0\n\t"
    "s_mov_b32 s63, 0\n\t"
    "s_mov_b32 s64, 0x400000\n\t"
    "v_mov_b32 v170, -1\n\t"
    "v_mov_b32 v171, -1\n\t"
    "v_mov_b32 v172, -1\n\t"
    "v_mov_b32 v173, -1\n\t"
    "s_mov_b32 s65, 589824\n\t"
    "buffer_store_dwordx4 v[170:173], %[addr], %[rw], s65 offen offset:0 sc1\n\t"
    "buffer_store_dwordx4 v[170:173], %[addr], %[rw], s65 offen offset:1024 sc1\n\t"
    "buffer_store_dwordx4 v[170:173], %[addr], %[rw], s65 offen offset:2048 sc1\n\t"
    "buffer_store_dwordx4 v[170:173], %[addr], %[rw], s65 offen offset:3072 sc1\n\t"
    "s_mov_b32 s65, 262144\n\t"
    "buffer_store_dwordx2 v[170:171], %[maddr], %[rw], s65 offen sc1\n\t"
    "RTOP_%=:\n\t"
    "s_waitcnt vmcnt(0)\n\t"
    "s_mov_b32 exec_lo, 0x00010001\n\t"
    "s_mov_b32 exec_hi, 0x00010001\n\t"
    "RP_%=:\n\t"
    "buffer_load_dword v168, %[addr], %[rw], s60 offen offset:2060 sc1\n\t"
    "s_waitcnt vmcnt(0)\n\t"
    "v_cmp_ne_u32 vcc, -1, v168\n\t"
    "s_cbranch_vccnz RPD_%=\n\t"
    "s_sleep 1\n\t"
    "s_sub_u32 s64, s64, 1\n\t"
    "s_cmp_lg_u32 s64, 0\n\t"
    "s_cbranch_scc1 RP_%=\n\t"
    "RPD_%=:\n\t"
    "s_mov_b64 exec, -1\n\t"
    "buffer_load_dwordx4 v[100:103], %[addr], %[rw], s60 offen offset:0 sc1\n\t"
    "buffer_load_dwordx4 v[104:107], %[addr], %[rw], s60 offen offset:1024 sc1\n\t"
    "buffer_load_dwordx4 v[108:111], %[addr], %[rw], s60 offen offset:2048 sc1\n\t"
    "buffer_load_dwordx4 v[112:115], %[addr], %[rw], s60 offen offset:3072 sc1\n\t"
    "s_sleep 1\n\t"
    "buffer_load_dwordx4 v[116:119], %[addr], %[rw], s60 offen offset:0 sc1\n\t"
    "buffer_load_dwordx4 v[120:123], %[addr], %[rw], s60 offen offset:1024 sc1\n\t"
    "buffer_load_dwordx4 v[124:127], %[addr], %[rw], s60 offen offset:2048 sc1\n\t"
    "buffer_load_dwordx4 v[128:131], %[addr], %[rw], s60 offen offset:3072 sc1\n\t"
    "s_sleep 1\n\t"
    "buffer_load_dwordx4 v[132:135], %[addr], %[rw], s60 offen offset:0 sc1\n\t"
    "buffer_load_dwordx4 v[136:139], %[addr], %[rw], s60 offen offset:1024 sc1\n\t"
    "buffer_load_dwordx4 v[140:143], %[addr], %[rw], s60 offen offset:2048 sc1\n\t"
    "buffer_load_dwordx4 v[144:147], %[addr], %[rw], s60 offen offset:3072 sc1\n\t"
    "RL_%=:\n\t"
    "s_waitcnt vmcnt(8)\n\t"
    "v_mfma_f32_16x16x32_bf16 v[164:167], %[sa], v[100:103], 0\n\t"
    "v_max3_u32 v168, v100, v101, v102\n\t"
    "v_max3_u32 v169, v103, v104, v105\n\t"
    "v_mfma_f32_16x16x32_bf16 v[164:167], %[sa], v[104:107], v[164:167]\n\t"
    "v_max3_u32 v168, v168, v106, v107\n\t"
    "v_max3_u32 v169, v169, v108, v109\n\t"
    "v_mfma_f32_16x16x32_bf16 v[164:167], %[sa], v[108:111], v[164:167]\n\t"
    "v_max3_u32 v168, v168, v110, v111\n\t"
    "v_max3_u32 v169, v169, v112, v113\n\t"
    "v_mfma_f32_16x16x32_bf16 v[164:167], %[sa], v[112:115], v[164:167]\n\t"
    "v_max3_u32 v168, v168, v114, v115\n\t"
    "v_max_u32 v168, v168, v169\n\t"
    "v_cmp_eq_u32 vcc, -1, v168\n\t"
    "s_cbranch_vccz ROK_%=\n\t"
    "s_sleep 1\n\t"
    "s_nop 4\n\t"
    "buffer_load_dwordx4 v[100:103], %[addr], %[rw], s60 offen offset:0 sc1\n\t"
    "buffer_load_dwordx4 v[104:107], %[addr], %[rw], s60 offen offset:1024 sc1\n\t"
    "buffer_load_dwordx4 v[108:111], %[addr], %[rw], s60 offen offset:2048 sc1\n\t"
    "buffer_load_dwordx4 v[112:115], %[addr], %[rw], s60 offen offset:3072 sc1\n\t"
    "s_waitcnt vmcnt(8)\n\t"
    "v_mfma_f32_16x16x32_bf16 v[164:167], %[sa], v[116:119], 0\n\t"
    "v_max3_u32 v168, v116, v117, v118\n\t"
    "v_max3_u32 v169, v119, v120, v121\n\t"
    "v_mfma_f32_16x16x32_bf16 v[164:167], %[sa], v[120:123], v[164:167]\n\t"
    "v_max3_u32 v168, v168, v122, v123\n\t"
    "v_max3_u32 v169, v169, v124, v125\n\t"
    "v_mfma_f32_16x16x32_bf16 v[164:167], %[sa], v[124:127], v[164:167]\n\t"
    "v_max3_u32 v168, v168, v126, v127\n\t"
    "v_max3_u32 v169, v169, v128, v129\n\t"
    "v_mfma_f32_16x16x32_bf16 v[164:167], %[sa], v[128:131], v[164:167]\n\t"
    "v_max3_u32 v168, v168, v130, v131\n\t"
    "v_max_u32 v168, v168, v169\n\t"
    "v_cmp_eq_u32 vcc, -1, v168\n\t"
    "s_cbranch_vccz ROK_%=\n\t"
    "s_sleep 1\n\t"
    "s_nop 4\n\t"
    "buffer_load_dwordx4 v[116:119], %[addr], %[rw], s60 offen offset:0 sc1\n\t"
    "buffer_load_dwordx4 v[120:123], %[addr], %[rw], s60 offen offset:1024 sc1\n\t"
    "buffer_load_dwordx4 v[124:127], %[addr], %[rw], s60 offen offset:2048 sc1\n\t"
    "buffer_load_dwordx4 v[128:131], %[addr], %[rw], s60 offen offset:3072 sc1\n\t"
    "s_waitcnt vmcnt(8)\n\t"
    "v_mfma_f32_16x16x32_bf16 v[164:167], %[sa], v[132:135], 0\n\t"
    "v_max3_u32 v168, v132, v133, v134\n\t"
    "v_max3_u32 v169, v135, v136, v137\n\t"
    "v_mfma_f32_16x16x32_bf16 v[164:167], %[sa], v[136:139], v[164:167]\n\t"
    "v_max3_u32 v168, v168, v138, v139\n\t"
    "v_max3_u32 v169, v169, v140, v141\n\t"
    "v_mfma_f32_16x16x32_bf16 v[164:167], %[sa], v[140:143], v[164:167]\n\t"
    "v_max3_u32 v168, v168, v142, v143\n\t"
    "v_max3_u32 v169, v169, v144, v145\n\t"
    "v_mfma_f32_16x16x32_bf16 v[164:167], %[sa], v[144:147], v[164:167]\n\t"
    "v_max3_u32 v168, v168, v146, v147\n\t"
    "v_max_u32 v168, v168, v169\n\t"
    "v_cmp_eq_u32 vcc, -1, v168\n\t"
    "s_cbranch_vccz ROK_%=\n\t"
    "s_sleep 1\n\t"
    "s_nop 4\n\t"
    "buffer_load_dwordx4 v[132:135], %[addr], %[rw], s60 offen offset:0 sc1\n\t"
    "buffer_load_dwordx4 v[136:139], %[addr], %[rw], s60 offen offset:1024 sc1\n\t"
    "buffer_load_dwordx4 v[140:143], %[addr], %[rw], s60 offen offset:2048 sc1\n\t"
    "buffer_load_dwordx4 v[144:147], %[addr], %[rw], s60 offen offset:3072 sc1\n\t"
    "s_sub_u32 s64, s64, 1\n\t"
    "s_cmp_lg_u32 s64, 0\n\t"
    "s_cbranch_scc1 RL_%=\n\t"
    "s_branch RTMO_%=\n\t"
    "ROK_%=:\n\t"
    "s_nop 5\n\t"
    "v_add_f32_dpp v164, v164, v164 row_ror:8 row_mask:0xf bank_mask:0xf\n\t"
    "v_add_f32_dpp v165, v165, v165 row_ror:8 row_mask:0xf bank_mask:0xf\n\t"
    "v_add_f32_dpp v166, v166, v166 row_ror:8 row_mask:0xf bank_mask:0xf\n\t"
    "v_add_f32_dpp v167, v167, v167 row_ror:8 row_mask:0xf bank_mask:0xf\n\t"
    "v_cvt_pk_bf16_f32 v168, v164, v165\n\t"
    "v_cvt_pk_bf16_f32 v169, v166, v167\n\t"
    "buffer_store_dwordx2 v[168:169], %[maddr], %[rw], s61 offen sc1\n\t"
    "s_waitcnt vmcnt(1)\n\t"
    "s_sleep 16\n\t"
    "buffer_store_dwordx4 v[170:173], %[addr], %[rw], s60 offen offset:0 sc1\n\t"
    "buffer_store_dwordx4 v[170:173], %[addr], %[rw], s60 offen offset:1024 sc1\n\t"
    "buffer_store_dwordx4 v[170:173], %[addr], %[rw], s60 offen offset:2048 sc1\n\t"
    "buffer_store_dwordx4 v[170:173], %[addr], %[rw], s60 offen offset:3072 sc1\n\t"
    "buffer_store_dwordx2 v[170:171], %[maddr], %[rw], s62 offen sc1\n\t"
    "s_xor_b32 s60, s60, 589824\n\t"
    "s_mov_b32 s62, s61\n\t"
    "s_add_u32 s61, s61, 131072\n\t"
    "s_cmp_eq_u32 s61, 393216\n\t"
    "s_cselect_b32 s61, 0, s61\n\t"
    "s_add_u32 s63, s63, 1\n\t"
    "s_cmp_lg_u32 s63, 63\n\t"
    "s_cbranch_scc1 RTOP_%=\n\t"
    "s_branch ROUT_%=\n\t"
    "RTMO_%=:\n\t"
    "v_mov_b32 v168, 0\n\t"
    "v_mov_b32 v169, 1\n\t"
    "s_mov_b32 s60, 2097216\n\t"
    "s_nop 1\n\t"
    "buffer_store_dword v169, v168, %[rw], s60 offen sc1\n\t"
    "ROUT_%=:\n\t"
    "s_waitcnt vmcnt(0)\n\t"
    :: [rw] "s"(rw), [addr] "v"(addr), [maddr] "v"(maddr), [sa] "v"(sa), [dbg] "s"(dbg)
    : "memory", "vcc", "scc", "v100", "v101", "v102", "v103", "v104", "v105", "v106", "v107", "v108", "v109", "v110", "v111", "v112", "v113", "v114", "v115", "v116", "v117", "v118", "v119", "v120", "v121", "v122", "v123", "v124", "v125", "v126", "v127", "v128", "v129", "v130", "v131", "v132", "v133", "v134", "v135", "v136", "v137", "v138", "v139", "v140", "v141", "v142", "v143", "v144", "v145", "v146", "v147", "v148", "v149", "v150", "v151", "v152", "v153", "v154", "v155", "v156", "v157", "v158", "v159", "v160", "v161", "v162", "v163", "v164", "v165", "v166", "v167", "v168", "v169", "v170", "v171", "v172", "v173", "s60", "s61", "s62", "s63", "s64", "s65", "s66", "s67", "s68", "s69", "s70", "s71");
}

__device__ void role_head(const Params& p, int h, int wave, int slot) {
    head_asm(mk_rsrc(p.wq, 576 * 4096 * 2), mk_rsrc(p.wk, 576 * 4096 * 2), mk_rsrc(p.wo, 576 * 4096 * 2), mk_rsrc(p.ws, p.wsbytes), h, wave, slot);
    __builtin_unreachable();
}
__device__ void role_vcu(const Params& p, int h, int wave, int slot) {
    vcu_asm(mk_rsrc(p.wv, 576 * 4096 * 2), mk_rsrc(p.ws, p.wsbytes), h, wave, slot);
    __builtin_unreachable();
}
__device__ void role_red(const Params& p, int u, int lane, unsigned sel) {
    v4u sa = {0u, 0u, 0u, 0u};
    { const int j = lane & 7; const unsigned one = (j & 1) ? 0x3f800000u : 0x00003f80u; sa.x = (j >> 1) == 0 ? one : 0u; sa.y = (j >> 1) == 1 ? one : 0u; sa.z = (j >> 1) == 2 ? one : 0u; sa.w = (j >> 1) == 3 ? one : 0u; }
    const unsigned addr = sel + (u >> 1) * 16384 + (u & 1) * 4096 + lane * 16;
    const int rr = ((lane >> 3) & 1) + 2 * (lane >> 5);
    const unsigned maddr = sel + MBASE + rr * 32768 + (u < 64 ? u * 128 : 16384 + (u - 64) * 128) + (lane & 7) * 16 + ((lane >> 4) & 1) * 8;
    red_asm(mk_rsrc(p.ws, p.wsbytes), addr, maddr, sa, 0x7500000u + u * 512);
}

__device__ __forceinline__ void prefetch_head_w(const Params& p, int x, int j, int lane) {
    const rsrc_t rq = mk_rsrc(p.wq, 576 * 4096 * 2), rk = mk_rsrc(p.wk, 576 * 4096 * 2), rv = mk_rsrc(p.wv, 576 * 4096 * 2), ro = mk_rsrc(p.wo, 576 * 4096 * 2);
    const unsigned oa = (unsigned)(j >> 1) * 8192u + (unsigned)(x + 4 * ((j & 1) * 4 + (lane >> 4))) * 256u + (lane & 15) * 16;
    const unsigned ob = (unsigned)(x + 4 * (j & 7)) * 147456u + (unsigned)(j >> 3) * 1024u + lane * 16;
#pragma unroll 1
    for (int half = 0; half < 2; ++half) {
        v4u v[36];
#pragma unroll
        for (int i = 0; i < 18; ++i) {
            v[2 * i] = __builtin_amdgcn_raw_buffer_load_b128(half ? ro : rv, (half ? ob : oa) + i * (half ? 8192u : 262144u), 0, 0);
            v[2 * i + 1] = __builtin_amdgcn_raw_buffer_load_b128(half ? rq : rk, oa + i * 262144u, 0, 0);
        }
#pragma unroll
        for (int i = 0; i < 36; ++i) asm volatile("" :: "v"(v[i]));
    }
}

__device__ __forceinline__ void load_w128(const unsigned short* W, unsigned col0, int w2, int lane) {
    const rsrc_t rs = mk_rsrc(W, 576 * 4096 * 2);
    const int row = 4 * w2 + (lane >> 4), u16 = lane & 15;
    const unsigned off = (unsigned)row * 8192u + (unsigned)((((u16 >> 1) ^ (row & 7)) * 16 + (u16 & 1) * 8) * 2) + col0 * 2u;
#pragma unroll 1
    for (int i = 0; i < 72; ++i) {
        asm volatile("s_mov_b32 m0, %0\n\ts_nop 0\n\tbuffer_load_dwordx4 %1, %2, %3 offen lds" :: "s"((i * 2 + w2) * 1024), "v"(off), "s"(rs), "s"(i * 65536) : "memory");
        if (i == 59) asm volatile("s_waitcnt vmcnt(40)" ::: "memory");
    }
}
__device__ __forceinline__ v4u wfrag128(unsigned char* lds, int ks, int nt, int lane) {
    const int rr = lane >> 2, pp = lane & 3;
    const unsigned a = (unsigned)(rr * 256 + ((nt ^ (rr & 7)) * 32) + pp * 8 + ks * 8192);
    const v2u lo = trread(lds, a), hi = trread(lds, a + 4096);
    return (v4u){lo.x, lo.y, hi.x, hi.y};
}
__device__ __forceinline__ void build_xf(const Params& p, unsigned char* lds, int seg, const v4u (&v)[9]) {
    const rsrc_t rw = mk_rsrc(p.ws, p.wsbytes);
#pragma unroll
    for (int i = 0; i < 9; ++i) {
        const int u = i * 256 + threadIdx.x, tok = u / 72, pc = u & 3, ks = (u - tok * 72) >> 2;
        unsigned char* d = lds + (((tok >> 4) * 18 + ks) * 64 + (tok & 15) + (pc & 1) * 32) * 16 + (pc >> 1) * 8;
        *(v2u*)d = (v2u){v[i].x, v[i].y};
        *(v2u*)(d + 256) = (v2u){v[i].z, v[i].w};
    }
    __syncthreads();
#pragma unroll
    for (int i = 0; i < 9; ++i) st_sc1(rw, OFF_XF + (unsigned)seg * 36864 + (i * 256 + threadIdx.x) * 16, *(const v4u*)(lds + (i * 256 + threadIdx.x) * 16));
}
__device__ __forceinline__ bool wait_xf(const Params& p, int first, int stride, int lane) {
    gu32* w = (gu32*)(p.ws + OFF_HS + HS_X + (first + stride * (lane & 15)) * 64);
    const unsigned n = __hip_atomic_load((gu32*)(p.ws + OFF_HS + HS_B), RLX_AGENT);
    for (unsigned spins = 0;; ++spins) {
        if (__all(__hip_atomic_load(w, RLX_AGENT) == n)) break;
        __builtin_amdgcn_s_sleep(3);
        if (spins > (1u << 21)) { set_tmo(p.ws); return false; }
    }
    __builtin_amdgcn_fence(__ATOMIC_ACQUIRE, "wavefront");
    return true;
}
__device__ __forceinline__ unsigned xf_off(int tile, int lane) { return OFF_XF + (unsigned)tile * 18432u + lane * 16; }
__device__ __forceinline__ void load_xfrags(rsrc_t rw, unsigned off, v4u (&xf)[2][18]) {
#pragma unroll
    for (int t = 0; t < 2; ++t)
#pragma unroll
        for (int ks = 0; ks < 18; ++ks) xf[t][ks] = ld_sc1(rw, off + (t * 18 + ks) * 1024);
}
#define ILV16() _Pragma("unroll") for (int i_ = 0; i_ < 16; ++i_) { __builtin_amdgcn_sched_group_barrier(0x8, 1, 0); __builtin_amdgcn_sched_group_barrier(0x100, 1, 0); }
template <bool WA>
__device__ __forceinline__ void gemm32x128(unsigned char* lds, int lane, v4u (&xf)[2][18], f32x4 (&acc)[2][8], rsrc_t rw, unsigned xnext) {
#pragma unroll
    for (int t = 0; t < 2; ++t)
#pragma unroll
        for (int n = 0; n < 8; ++n) acc[t][n] = (f32x4){0.f, 0.f, 0.f, 0.f};
    v4u wa[8], wb[8];
#pragma unroll
    for (int n = 0; n < 8; ++n) wa[n] = wfrag128(lds, 0, n, lane);
    __builtin_amdgcn_sched_barrier(0);
#pragma unroll
    for (int ks = 0; ks < 18; ks += 2) {
#pragma unroll
        for (int n = 0; n < 8; ++n) wb[n] = wfrag128(lds, ks + 1, n, lane);
#pragma unroll
        for (int n = 0; n < 8; ++n) {
            acc[0][n] = WA ? mfma32(wa[n], xf[0][ks], acc[0][n]) : mfma32(xf[0][ks], wa[n], acc[0][n]);
            acc[1][n] = WA ? mfma32(wa[n], xf[1][ks], acc[1][n]) : mfma32(xf[1][ks], wa[n], acc[1][n]);
        }
        ILV16();
        __builtin_amdgcn_sched_barrier(0);
        xf[0][ks] = ld_sc1(rw, xnext + ks * 1024); xf[1][ks] = ld_sc1(rw, xnext + (18 + ks) * 1024);
        __builtin_amdgcn_sched_barrier(0);
        if (ks + 2 < 18) {
#pragma unroll
            for (int n = 0; n < 8; ++n) wa[n] = wfrag128(lds, ks + 2, n, lane);
        }
#pragma unroll
        for (int n = 0; n < 8; ++n) {
            acc[0][n] = WA ? mfma32(wb[n], xf[0][ks + 1], acc[0][n]) : mfma32(xf[0][ks + 1], wb[n], acc[0][n]);
            acc[1][n] = WA ? mfma32(wb[n], xf[1][ks + 1], acc[1][n]) : mfma32(xf[1][ks + 1], wb[n], acc[1][n]);
        }
        ILV16();
        __builtin_amdgcn_sched_barrier(0);
        xf[0][ks + 1] = ld_sc1(rw, xnext + (ks + 1) * 1024); xf[1][ks + 1] = ld_sc1(rw, xnext + (19 + ks) * 1024);
        __builtin_amdgcn_sched_barrier(0);
    }
}
__device__ __forceinline__ v4u pk8(const f32x4& a, const f32x4& b) { return (v4u){pk2(a[0], a[1]), pk2(a[2], a[3]), pk2(b[0], b[1]), pk2(b[2], b[3])}; }

template <bool ISV>
__device__ __forceinline__ void kv_loop(const Params& p, unsigned char* lds, int h, int wave, int lane, int slot) {
    const rsrc_t rw = mk_rsrc(p.ws, p.wsbytes);
    v4u xf[2][18] = {};
    unsigned hc = wave < 2 ? hs_begin(p.ws, slot) : 0u;
    for (int rb = -1; rb < 16; ++rb) {
        const int s = rb * 4 + wave;
        f32x4 acc[2][8];
        gemm32x128<!ISV>(lds, lane, xf, acc, rw, rb < 0 ? 0xC0000000u : xf_off(((rb + 1) & 15) * 8 + wave * 2, lane));
        if (rb < 0) {
            asm volatile("" :: "v"(acc[0][0]), "v"(acc[1][7]));
            if (wave == 0) TL(6, 1024 + (h * 2 + ISV) * 8);
            if (wave >= 2) { asm volatile("s_waitcnt vmcnt(0)" ::: "memory"); __builtin_amdgcn_s_barrier(); asm volatile("" ::: "memory"); hc = hs_begin(p.ws, slot); }
            if (!hs_wait<false>(p.ws, slot, hc) || !wait_xf(p, wave, 4, lane)) return;
            if (wave == 0) TL(6, 1024 + (h * 2 + ISV) * 8 + 2);
            load_xfrags(rw, xf_off(wave * 2, lane), xf);
            if (wave < 2) { __builtin_amdgcn_s_barrier(); asm volatile("" ::: "memory"); }
            if (wave == 0) TL(6, 1024 + (h * 2 + ISV) * 8 + 3);
            continue;
        }
        if (rb == 0 && wave == 0) TL(6, 1024 + (h * 2 + ISV) * 8 + 4);
        const unsigned base = (ISV ? OFF_VF : OFF_KF) + (unsigned)(s * 32 + h) * 8192u + lane * 16;
        if (ISV) {
#pragma unroll
            for (int n = 0; n < 8; ++n) st_sc1(rw, base + n * 1024, pk8(acc[0][n], acc[1][n]));
        } else {
#pragma unroll
            for (int t = 0; t < 2; ++t)
#pragma unroll
                for (int kk = 0; kk < 4; ++kk) st_sc1(rw, base + (t * 4 + kk) * 1024, pk8(acc[t][2 * kk], acc[t][2 * kk + 1]));
        }
        DRAIN();
        if (rb == 8 && !wait_deferred(p.ws, lane)) return;
        TL(1, s * 64 + h * 2 + ISV);
        if (lane == 0) __hip_atomic_store((gu32*)(p.ws + OFF_CTRL + C_KVF + (s * 32 + h) * 64 + (ISV ? 4 : 0)), 1u, RLX_AGENT);
    }
}
__device__ void role_kv(const Params& p, unsigned char* lds, int cb, int wave, int lane, int slot) {
    const int h = cb >> 1, isv = cb & 1;
    if (wave >= 2) load_w128(isv ? p.wv : p.wk, h * 128, wave - 2, lane);
    if (isv) kv_loop<true>(p, lds, h, wave, lane, slot); else kv_loop<false>(p, lds, h, wave, lane, slot);
}

__device__ __forceinline__ void load_w144(unsigned ldsdst, rsrc_t rs, unsigned soff, int ldw, int ni, int wave, int lane) {
    int ln = lane; asm volatile("" : "+v"(ln), "+s"(wave));
    const int idx = wave * 64 + ln; int row = idx / 18, u = idx - row * 18;
    unsigned off = (unsigned)(row * ldw + u * 8) * 2u;
#pragma unroll 1
    for (int i = wave; i < ni; i += 4) {
        asm volatile("s_mov_b32 m0, %0\n\ts_nop 0\n\tbuffer_load_dwordx4 %1, %2, %3 offen lds" :: "s"(ldsdst + (unsigned)i * 1024u), "v"(off), "s"(rs), "s"(soff) : "memory");
        u += 4; off += (unsigned)(14 * ldw + 32) * 2u;
        if (u >= 18) { u -= 18; off += (unsigned)(ldw - 144) * 2u; }
    }
}
__device__ __forceinline__ v4u wfrag144(unsigned char* lds, unsigned base, int ks, int nt, int lane) {
    const unsigned a = base + (unsigned)((lane >> 2) * 288 + (lane & 3) * 8 + ks * 9216 + nt * 32);
    const v2u lo = trread(lds, a), hi = trread(lds, a + 4608);
    return (v4u){lo.x, lo.y, hi.x, hi.y};
}
__device__ __forceinline__ float xmax4(float v) {
    auto a = __builtin_amdgcn_permlane16_swap(__float_as_uint(v), __float_as_uint(v), false, false); v = fmaxf(__uint_as_float(a[0]), __uint_as_float(a[1]));
    auto b = __builtin_amdgcn_permlane32_swap(__float_as_uint(v), __float_as_uint(v), false, false); return fmaxf(__uint_as_float(b[0]), __uint_as_float(b[1]));
}
__device__ __forceinline__ float xsum4(float v) {
    auto a = __builtin_amdgcn_permlane16_swap(__float_as_uint(v), __float_as_uint(v), false, false); v = __uint_as_float(a[0]) + __uint_as_float(a[1]);
    auto b = __builtin_amdgcn_permlane32_swap(__float_as_uint(v), __float_as_uint(v), false, false); return __uint_as_float(b[0]) + __uint_as_float(b[1]);
}
__device__ __forceinline__ unsigned mx4(v4u v) { return max(max(v.x, v.y), max(v.z, v.w)); }
__device__ __forceinline__ bool fresh_or_dead(unsigned m, unsigned& spins, unsigned char* ws) {
    if (__all(m != ~0u)) return true;
    if (++spins > (1u << 20)) { set_tmo(ws); return true; }
    __builtin_amdgcn_s_sleep(1);
    asm volatile("" ::: "memory");
    return false;
}
#define BAR() do { asm volatile("s_waitcnt vmcnt(0) lgkmcnt(0)" ::: "memory"); __builtin_amdgcn_s_barrier(); asm volatile("" ::: "memory"); } while (0)

__device__ void role_att(const Params& p, unsigned char* lds, int h, int wave, int lane) {
    lane = fresh_lane();
    if (wave >= 2) load_w128(p.wq, h * 128, wave - 2, lane);
    const rsrc_t rw = mk_rsrc(p.ws, p.wsbytes);
    v4u xf[2][18];
    if (!wait_xf(p, wave, 4, lane)) return;
    load_xfrags(rw, xf_off(wave * 2, lane), xf);
    BAR();
    const int G = lane >> 4, c = lane & 15;
    constexpr float CS = 0.08838834764831845f * 1.4426950408889634f;
    v4u qf[2][4], qn[2][4] = {};
    for (int rb = -1; rb < 16; ++rb) {
        const int s = rb * 4 + wave;
#pragma unroll
        for (int i = 0; i < 8; ++i) qf[i >> 2][i & 3] = qn[i >> 2][i & 3];
        if (rb < 15) {
            f32x4 acc[2][8];
            gemm32x128<true>(lds, lane, xf, acc, rw, xf_off(((rb + 2) & 15) * 8 + wave * 2, lane));
#pragma unroll
            for (int t = 0; t < 2; ++t)
#pragma unroll
                for (int kk = 0; kk < 4; ++kk) qn[t][kk] = pk8(acc[t][2 * kk], acc[t][2 * kk + 1]);
        }
        if (rb < 0) continue;
        const unsigned sh = (unsigned)(s * 32 + h);
        if (!wait_ge(p.ws, OFF_CTRL + C_KVF + sh * 64, 1) || !wait_ge(p.ws, OFF_CTRL + C_KVF + sh * 64 + 4, 1)) return;
        v4u kf[2][4], vf[8];
#pragma unroll
        for (int i = 0; i < 8; ++i) kf[i >> 2][i & 3] = ld_sc1(rw, OFF_KF + sh * 8192 + i * 1024 + lane * 16);
#pragma unroll
        for (int i = 0; i < 8; ++i) vf[i] = ld_sc1(rw, OFF_VF + sh * 8192 + i * 1024 + lane * 16);
        f32x4 T[2][2];
        const f32x4 z4 = {0.f, 0.f, 0.f, 0.f};
        T[0][0] = z4; T[1][0] = z4; T[1][1] = z4;
#pragma unroll
        for (int kk = 0; kk < 4; ++kk) { T[0][0] = mfma32(kf[0][kk], qf[0][kk], T[0][0]); T[1][0] = mfma32(kf[0][kk], qf[1][kk], T[1][0]); T[1][1] = mfma32(kf[1][kk], qf[1][kk], T[1][1]); }
        if (!wait_nz8(p.ws, OFF_CTRL + C_KMC + (s < FAST0 ? sh : sh - 32) * 64, lane)) return;
        TL(7, sh);
        v4u km[4], vm[4];
        for (unsigned spins = 0;;) {
#pragma unroll
            for (int i = 0; i < 4; ++i) km[i] = ld_sc1(rw, OFF_KM + sh * 4096 + i * 1024 + lane * 16);
#pragma unroll
            for (int i = 0; i < 4; ++i) vm[i] = ld_sc1(rw, OFF_VM + sh * 4096 + i * 1024 + lane * 16);
            __builtin_amdgcn_sched_barrier(0);
            if (s < FAST0) break;
            unsigned m = 0;
#pragma unroll
            for (int i = 0; i < 4; ++i) m = max(m, max(mx4(km[i]), mx4(vm[i])));
            if (fresh_or_dead(m, spins, p.ws)) break;
        }
#pragma unroll
        for (int t = 0; t < 2; ++t) {
            f32x4 T2 = z4;
#pragma unroll
            for (int kk = 0; kk < 4; ++kk) T2 = mfma32(km[kk], qf[t][kk], T2);
            const float m0 = T2[0], m1 = T2[1];
            float d[4], o[4];
            float mx = fmaxf(m0, m1);
#pragma unroll
            for (int r = 0; r < 4; ++r) { d[r] = (4 * G + r <= c) ? T[t][t][r] : -INFINITY; mx = fmaxf(mx, d[r]); }
            if (t == 1) {
#pragma unroll
                for (int r = 0; r < 4; ++r) { o[r] = T[1][0][r]; mx = fmaxf(mx, o[r]); }
            }
            mx = xmax4(mx) * CS;
            const float p0 = rbf(__builtin_amdgcn_exp2f(m0 * CS - mx)), p1 = rbf(__builtin_amdgcn_exp2f(m1 * CS - mx));
            float l = p0 + p1;
#pragma unroll
            for (int r = 0; r < 4; ++r) { d[r] = rbf(__builtin_amdgcn_exp2f(d[r] * CS - mx)); l += d[r]; }
            v4u px;
            if (t == 1) {
#pragma unroll
                for (int r = 0; r < 4; ++r) { o[r] = rbf(__builtin_amdgcn_exp2f(o[r] * CS - mx)); l += o[r]; }
                px = (v4u){pk2(o[0], o[1]), pk2(o[2], o[3]), pk2(d[0], d[1]), pk2(d[2], d[3])};
            } else px = (v4u){pk2(d[0], d[1]), pk2(d[2], d[3]), 0u, 0u};
            const float inv = __builtin_amdgcn_rcpf(xsum4(l));
            const unsigned pm = pk2(p0, p1); const v4u p2 = {pm, pm, 0u, 0u}, p3 = {0u, 0u, pm, pm};
#pragma unroll
            for (int kk = 0; kk < 4; ++kk) {
                f32x4 oa = mfma32(vf[2 * kk], px, z4), ob = mfma32(vf[2 * kk + 1], px, z4);
                oa = mfma32(vm[kk], p2, oa); ob = mfma32(vm[kk], p3, ob);
                st_sc1(rw, OFF_OF + sh * 8192 + (t * 4 + kk) * 1024 + lane * 16, pk8(oa * inv, ob * inv));
            }
        }
        DRAIN();
        TL(2, sh);
        if (lane == 0) __hip_atomic_store((gu32*)(p.ws + OFF_CTRL + C_OFL + (sh >> 2) * 64 + (sh & 3) * 4), 1u, RLX_AGENT);
    }
}

template <int NT>
__device__ __forceinline__ void wo_loop(const Params& p, unsigned char* lds, int kq, int nb, int t, int n0, int lane) {
    const rsrc_t rw = mk_rsrc(p.ws, p.wsbytes);
    v4u wf[16][NT];
#pragma unroll
    for (int ks = 0; ks < 16; ++ks)
#pragma unroll
        for (int n = 0; n < NT; ++n) wf[ks][n] = wfrag144(lds, 0, ks, n0 + n, lane);
    const unsigned ob = OFF_OF + (unsigned)(kq * 4) * 8192 + t * 4096 + lane * 16;
    gu32* const fl = (gu32*)(p.ws + OFF_CTRL + C_WFL + (unsigned)(nb * 2 + t) * 512 + kq * 64 + (NT == 4) * 4);
    bool ready = false;
    for (int s = 0; s < 64; ++s) {
        f32x4 acc[NT];
#pragma unroll
        for (int n = 0; n < NT; ++n) acc[n] = (f32x4){0.f, 0.f, 0.f, 0.f};
        bool pend = s > 0;
        if (s < FAST0 && !ready) {
            if (pend && lane == 0) __hip_atomic_store(fl + (s - 1) * 1024, 1u, RLX_AGENT);
            pend = false;
            if (!wait_nz4(p.ws, rw, OFF_CTRL + C_OFL + (unsigned)(s * 8 + kq) * 64)) return;
        }
        v4u of[16];
        for (unsigned spins = 0;;) {
#pragma unroll
            for (int i = 0; i < 16; ++i) of[i] = ld_sc1(rw, ob + (unsigned)s * 262144 + (i >> 2) * 8192 + (i & 3) * 1024);
            __builtin_amdgcn_sched_barrier(0);
            if (pend && lane == 0) __hip_atomic_store(fl + (s - 1) * 1024, 1u, RLX_AGENT);
            pend = false;
            __builtin_amdgcn_sched_barrier(0);
            if (s < FAST0) break;
            unsigned m = 0;
#pragma unroll
            for (int i = 0; i < 16; ++i) m = max(m, mx4(of[i]));
            if (fresh_or_dead(m, spins, p.ws)) break;
        }
#pragma unroll
        for (int ks = 0; ks < 16; ++ks)
#pragma unroll
            for (int n = 0; n < NT; ++n) acc[n] = mfma32(wf[ks][n], of[ks], acc[n]);
        const unsigned base = OFF_WP + (unsigned)(((s * 4 + nb) * 8 + kq) * 18 + t * 9 + n0) * 1024 + lane * 16;
#pragma unroll
        for (int n = 0; n < NT; ++n) st_sc1(rw, base + n * 1024, __builtin_bit_cast(v4u, acc[n]));
        const v4u nf = ld_sc1(rw, OFF_CTRL + C_OFL + (unsigned)(min(s + 1, 63) * 8 + kq) * 64);
        asm volatile("s_waitcnt vmcnt(0)" :: "v"(nf) : "memory");
        if (NT == 5) TL(3, s * 64 + (kq * 4 + nb) * 2 + t);
        ready = __builtin_amdgcn_readfirstlane(min(min(nf.x, nf.y), min(nf.z, nf.w))) != 0;
        __builtin_amdgcn_fence(__ATOMIC_ACQUIRE, "wavefront");
    }
}
__device__ void role_wo(const Params& p, unsigned char* lds, int idx, int wave, int lane) {
    const int kq = idx >> 2, nb = idx & 3;
    lane = fresh_lane();
    load_w144(0u, mk_rsrc(p.wo, 4096 * 576 * 2), (unsigned)(kq * 512 * 576 + nb * 144) * 2u, 576, 144, wave, lane);
    BAR();
    if (wave < 2) wo_loop<5>(p, lds, kq, nb, wave, 0, lane); else wo_loop<4>(p, lds, kq, nb, wave - 2, 5, lane);
}

__device__ __forceinline__ float phi(float a) { const float t = rbf(a); return rbf(rbf(fmaxf(t, 0.f) + __expf(fminf(t, 0.f))) + 1e-6f); }
constexpr unsigned CH_BYTES = 28 * 1024;
struct Stream {
    unsigned off[7]; rsrc_t rs; unsigned rowstep, colstep, isrc, irow, ibuf, rbuf, left;
    template <int I> __device__ __forceinline__ void piece(unsigned w1k) const {
        if (left) asm volatile("s_add_u32 m0, %3, %4\n\ts_add_u32 m0, m0, %5\n\ts_nop 0\n\tbuffer_load_dwordx4 %0, %1, %2 offen lds" :: "v"(off[I]), "s"(rs), "s"(isrc), "s"(w1k), "s"(ibuf), "i"(I * 4096) : "memory", "scc");
    }
    __device__ __forceinline__ void advance() {
        if (left) {
            --left;
            isrc += rowstep; if (++irow == 6) { irow = 0; isrc += colstep - 6 * rowstep; }
            ibuf += CH_BYTES; if (ibuf == 5 * CH_BYTES) ibuf = 0;
        }
    }
    __device__ __forceinline__ void issue(unsigned w1k) { piece<0>(w1k); piece<1>(w1k); piece<2>(w1k); piece<3>(w1k); piece<4>(w1k); piece<5>(w1k); piece<6>(w1k); advance(); }
    __device__ __forceinline__ void start(rsrc_t r, int ldw, unsigned src0, unsigned cstep, unsigned nchunks, int wave, int lane) {
#pragma unroll
        for (int i = 0; i < 7; ++i) { const int idx = min((i * 4 + wave) * 64 + lane, 96 * 18 - 1), row = idx / 18; off[i] = (unsigned)(row * ldw + (idx - row * 18) * 8) * 2u; }
        rs = r; rowstep = 96u * ldw * 2u; colstep = cstep; isrc = src0; irow = 0; ibuf = 0; rbuf = 0; left = nchunks;
        BAR();
        issue(wave * 1024u); issue(wave * 1024u); issue(wave * 1024u); issue(wave * 1024u);
    }
    __device__ __forceinline__ unsigned next(int wave) {
        asm volatile("s_waitcnt vmcnt(21) lgkmcnt(0)" ::: "memory");
        if (!left) asm volatile("s_waitcnt vmcnt(0)" ::: "memory");
        __builtin_amdgcn_s_barrier(); asm volatile("" ::: "memory");
        const unsigned r = rbuf; rbuf += CH_BYTES; if (rbuf == 5 * CH_BYTES) rbuf = 0;
        return r;
    }
};
template <int NTOK, bool WA>
__device__ __forceinline__ void stream_block(unsigned char* lds, Stream& st, int wave, int lane, const v4u (&xf)[NTOK][18], f32x4 (&acc)[NTOK][9]) {
#pragma unroll
    for (int t = 0; t < NTOK; ++t)
#pragma unroll
        for (int n = 0; n < 9; ++n) acc[t][n] = (f32x4){0.f, 0.f, 0.f, 0.f};
#pragma unroll
    for (int c = 0; c < 6; ++c) {
        const unsigned base = st.next(wave);
        v4u wf[3][9];
#pragma unroll
        for (int n = 0; n < 9; ++n) wf[0][n] = wfrag144(lds, base, 0, n, lane);
        __builtin_amdgcn_sched_barrier(0);
#define SB_GROUP(K3, G3, PI) do { \
            if (K3 < 2) { _Pragma("unroll") for (int n = 3 * G3; n < 3 * G3 + 3; ++n) wf[(K3 + 1) % 3][n] = wfrag144(lds, base, K3 + 1, n, lane); } \
            _Pragma("unroll") for (int n = 3 * G3; n < 3 * G3 + 3; ++n) _Pragma("unroll") for (int t = 0; t < NTOK; ++t) \
                acc[t][n] = WA ? mfma32(wf[K3][n], xf[t][c * 3 + K3], acc[t][n]) : mfma32(xf[t][c * 3 + K3], wf[K3][n], acc[t][n]); \
            _Pragma("unroll") for (int i_ = 0; i_ < 3 * NTOK; ++i_) { __builtin_amdgcn_sched_group_barrier(0x8, 1, 0); __builtin_amdgcn_sched_group_barrier(0x100, 2 / NTOK, 0); } \
            __builtin_amdgcn_sched_barrier(0); \
            if (PI < 7) st.template piece<(PI < 7 ? PI : 0)>(wave * 1024u); \
            __builtin_amdgcn_sched_barrier(0); } while (0)
        SB_GROUP(0, 0, 0); SB_GROUP(0, 1, 1); SB_GROUP(0, 2, 2); SB_GROUP(1, 0, 3); SB_GROUP(1, 1, 4); SB_GROUP(1, 2, 5); SB_GROUP(2, 0, 6); SB_GROUP(2, 1, 7); SB_GROUP(2, 2, 7);
        st.advance();
    }
}
__device__ __forceinline__ void task_b1(const Params& p, unsigned char* lds, int task, int wave) {
    const int lane = fresh_lane(); asm volatile("" : "+s"(wave));
    const int fb = task & 3, ch = task >> 2;
    const rsrc_t rw = mk_rsrc(p.ws, p.wsbytes);
    v4u xf[2][18];
    if (wave == 0) TL(5, 256 + task * 8);
    wait_xf(p, ch * 4 + wave, 0, lane);
    load_xfrags(rw, xf_off(ch * 8 + wave * 2, lane), xf);
    Stream st; st.start(mk_rsrc(p.wqkv, 576 * 1728 * 2), 1728, (576 + fb * 144) * 2u, 576 * 2u, 12, wave, lane);
    if (wave == 0) TL(5, 256 + task * 8 + 1);
    v4u fk[9], fv[9];
#pragma unroll 1
    for (int kv = 0; kv < 2; ++kv) {
        f32x4 acc[2][9];
        stream_block<2, false>(lds, st, wave, lane, xf, acc);
#pragma unroll
        for (int n = 0; n < 9; ++n) fk[n] = fv[n];
        if (kv == 0) {
#pragma unroll
            for (int n = 0; n < 9; ++n)
#pragma unroll
                for (int t = 0; t < 2; ++t)
#pragma unroll
                    for (int r = 0; r < 4; ++r) acc[t][n][r] = phi(acc[t][n][r]);
        }
#pragma unroll
        for (int n = 0; n < 9; ++n) fv[n] = pk8(acc[0][n], acc[1][n]);
    }
    BAR();
    if (wave == 0) TL(5, 256 + task * 8 + 5);
    f32x4* red = (f32x4*)lds + wave * 36 * 64 + lane;
    const f32x4 z4 = {0.f, 0.f, 0.f, 0.f}; const v4u ones = {0x3f803f80u, 0x3f803f80u, 0x3f803f80u, 0x3f803f80u};
#pragma unroll
    for (int i = 0; i < 9; ++i) {
        red[(i * 4 + 0) * 64] = i > 0 ? mfma32(fk[i], fv[i > 0 ? i - 1 : 0], z4) : z4;
        red[(i * 4 + 1) * 64] = mfma32(fk[i], fv[i], z4);
        red[(i * 4 + 2) * 64] = i < 8 ? mfma32(fk[i], fv[i < 8 ? i + 1 : 8], z4) : z4;
        red[(i * 4 + 3) * 64] = mfma32(fk[i], ones, z4);
    }
    BAR();
    const f32x4* rd = (const f32x4*)lds + lane;
#pragma unroll
    for (int i = 0; i < 9; ++i) {
        const int tl = wave * 9 + i;
        const f32x4 v = (rd[tl * 64] + rd[(36 + tl) * 64]) + (rd[(72 + tl) * 64] + rd[(108 + tl) * 64]);
        st_sc1(rw, OFF_B1P + (unsigned)((fb * 36 + tl) * 16 + ch) * 1024 + lane * 16, __builtin_bit_cast(v4u, v));
    }
    BAR();
    if (wave == 0) TL(5, task);
    if (threadIdx.x == 0) __hip_atomic_fetch_add((gu32*)(p.ws + OFF_CTRL + C_B1), 1u, RLX_AGENT);
}
__device__ __forceinline__ void task_b15(const Params& p, int unit, int lane) {
    const rsrc_t rw = mk_rsrc(p.ws, p.wsbytes);
    const int et = unit / 3, sr = unit - et * 3, i = et - 1 + sr;
    const int he = (16 * et + (lane & 15)) / 18;
    f32x4 a = {0.f, 0.f, 0.f, 0.f}, z = a;
    if (i >= 0 && i < 36 && i / 9 == et / 9) {
        const unsigned b0 = OFF_B1P + (unsigned)(i * 4 + 2 - sr) * 16384 + lane * 16, b1 = OFF_B1P + (unsigned)(i * 4 + 3) * 16384 + lane * 16;
        v4u va[16], vz[16];
#pragma unroll
        for (int c = 0; c < 16; ++c) { va[c] = ld_sc1(rw, b0 + c * 1024); vz[c] = ld_sc1(rw, b1 + c * 1024); }
#pragma unroll
        for (int c = 0; c < 16; ++c) { a += __builtin_bit_cast(f32x4, va[c]); z += __builtin_bit_cast(f32x4, vz[c]); }
#pragma unroll
        for (int r = 0; r < 4; ++r) if ((16 * i + 4 * (lane >> 4) + r) / 18 != he) { a[r] = 0.f; z[r] = 0.f; }
    }
    st_sc1(rw, OFF_NF + (unsigned)unit * 1024 + lane * 16, (v4u){pk2(a[0], a[1]), pk2(a[2], a[3]), pk2(z[0], z[1]), pk2(z[2], z[3])});
    DRAIN();
    TL(5, 64 + unit);
    if (lane == 0) __hip_atomic_fetch_add((gu32*)(p.ws + OFF_CTRL + C_B15), 1u, RLX_AGENT);
}
__device__ __forceinline__ bool task_b2(const Params& p, unsigned char* lds, int task, int wave) {
    const int lane = fresh_lane();
    const rsrc_t rw = mk_rsrc(p.ws, p.wsbytes);
    const int tile = task * 4 + wave;
    const f32x4 z4 = {0.f, 0.f, 0.f, 0.f};
    v4u af[1][18];
    if (!wait_xf(p, tile >> 1, 0, lane)) return false;
    if (wave == 0) TL(5, 1024 + task * 8);
#pragma unroll
    for (int ks = 0; ks < 18; ++ks) af[0][ks] = ld_sc1(rw, xf_off(tile, lane) + ks * 1024);
    Stream st;
    float gs = 0.f;
    {
        v2u gw0[18], gw1[18];
#pragma unroll
        for (int ks = 0; ks < 18; ++ks) { gw0[ks] = *(const v2u*)(p.gw + ks * 32 + 4 * (lane >> 4)); gw1[ks] = *(const v2u*)(p.gw + ks * 32 + 16 + 4 * (lane >> 4)); }
        __builtin_amdgcn_sched_barrier(0);
        st.start(mk_rsrc(p.wqkv, 576 * 1728 * 2), 1728, 0u, 288u, 24, wave, lane);
        __builtin_amdgcn_sched_barrier(0);
#pragma unroll
        for (int ks = 0; ks < 18; ++ks) {
            const unsigned xs4[4] = {af[0][ks].x, af[0][ks].y, af[0][ks].z, af[0][ks].w}, ws4[4] = {gw0[ks].x, gw0[ks].y, gw1[ks].x, gw1[ks].y};
#pragma unroll
            for (int i = 0; i < 4; ++i) gs += rbf(__uint_as_float(xs4[i] << 16) * __uint_as_float(ws4[i] << 16)) + rbf(__uint_as_float(xs4[i] & 0xffff0000u) * __uint_as_float(ws4[i] & 0xffff0000u));
        }
    }
    const float g = rbf(1.0f / (1.0f + __expf(-rbf(xsum4(gs)))));
    const float omg = rbf(1.0f - g);
    if (wave == 0) TL(5, 1024 + task * 8 + 1);
    v2u ob[36];
#pragma unroll 1
    for (int nb = 0; nb < 4; ++nb) {
        f32x4 acc[1][9];
        stream_block<1, true>(lds, st, wave, lane, af, acc);
#pragma unroll
        for (int i = 0; i < 27; ++i) ob[i] = ob[i + 9];
#pragma unroll
        for (int n = 0; n < 9; ++n) ob[27 + n] = (v2u){pk2(phi(acc[0][n][0]), phi(acc[0][n][1])), pk2(phi(acc[0][n][2]), phi(acc[0][n][3]))};
    }
    if (wave == 0) TL(5, 1024 + task * 8 + 2);
    st.start(mk_rsrc(p.wob, 576 * 576 * 2), 576, 0u, 288u, 24, wave, lane);
    if (!wait_ge(p.ws, OFF_CTRL + C_B15, 108)) return false;
#pragma unroll 1
    for (int nb = 0; nb < 4; ++nb) {
        v4u nf[27];
#pragma unroll
        for (int i = 0; i < 27; ++i) nf[i] = ld_sc1(rw, OFF_NF + (unsigned)(nb * 27 + i) * 1024 + lane * 16);
        __builtin_amdgcn_sched_barrier(0);
        v2u pq[9];
#pragma unroll
        for (int n = 0; n < 9; ++n) pq[n] = ob[n];
#pragma unroll
        for (int i = 0; i < 27; ++i) ob[i] = ob[i + 9];
#pragma unroll
        for (int e = 0; e < 9; ++e) {
            f32x4 num = z4, den = z4;
#pragma unroll
            for (int sr = 0; sr < 3; ++sr) {
                const int i = e - 1 + sr;
                if (i >= 0 && i < 9) { num = mfma16((v2u){nf[e * 3 + sr].x, nf[e * 3 + sr].y}, pq[i], num); den = mfma16((v2u){nf[e * 3 + sr].z, nf[e * 3 + sr].w}, pq[i], den); }
            }
            float o[4];
#pragma unroll
            for (int r = 0; r < 4; ++r) o[r] = rbf(num[r]) * __builtin_amdgcn_rcpf(rbf(rbf(den[r]) + 1e-6f));
            ob[27 + e] = (v2u){pk2(o[0], o[1]), pk2(o[2], o[3])};
        }
    }
#pragma unroll
    for (int ks = 0; ks < 18; ++ks) af[0][ks] = (v4u){ob[2 * ks].x, ob[2 * ks].y, ob[2 * ks + 1].x, ob[2 * ks + 1].y};
    if (wave == 0) TL(5, 1024 + task * 8 + 3);
#pragma unroll 1
    for (int nb = 0; nb < 4; ++nb) {
        f32x4 acc[1][9];
        stream_block<1, true>(lds, st, wave, lane, af, acc);
#pragma unroll
        for (int i = 0; i < 27; ++i) ob[i] = ob[i + 9];
#pragma unroll
        for (int n = 0; n < 9; ++n) ob[27 + n] = (v2u){pk2(omg * rbf(acc[0][n][0]), omg * rbf(acc[0][n][1])), pk2(omg * rbf(acc[0][n][2]), omg * rbf(acc[0][n][3]))};
    }
#pragma unroll
    for (int nt = 0; nt < 36; ++nt) __builtin_amdgcn_raw_buffer_store_b64(ob[nt], rw, OFF_BB + (unsigned)(tile * 36 + nt) * 512 + lane * 8, 0, 16);
    if (wave == 0) TL(5, 1024 + task * 8 + 4);
    if (lane < 16) __hip_atomic_store((gu32*)(p.ws + OFF_G + (tile * 16 + lane) * 4), __float_as_uint(g), RLX_AGENT);
    DRAIN();
    TL(6, tile);
    if (lane == 0) __hip_atomic_store((gu32*)(p.ws + OFF_CTRL + C_BBF + tile * 64), 1u, RLX_AGENT);
    return true;
}
__device__ void role_fin(const Params& p, int f, int wave) {
    const int lane = fresh_lane();
    {
        const rsrc_t rq = mk_rsrc(p.wqkv, 576 * 1728 * 2), ro = mk_rsrc(p.wob, 576 * 576 * 2);
        const int j = (f >> 2) * 4 + wave, idx = j * 64 + lane;
        v4u v[54];
#pragma unroll
        for (int i = 0; i < 27; ++i) { const int u = i * 1536 + idx, row = u / 72; v[i] = __builtin_amdgcn_raw_buffer_load_b128(rq, row * 3456 + (u - row * 72) * 16, 0, 0); }
#pragma unroll
        for (int i = 0; i < 27; ++i) v[27 + i] = __builtin_amdgcn_raw_buffer_load_b128(ro, idx * 16, i * 24576, 0);
#pragma unroll
        for (int i = 0; i < 54; ++i) asm volatile("" :: "v"(v[i]));
    }
    {
        if (!wait_ge(p.ws, OFF_CTRL + C_B1, 64)) return;
#pragma unroll 1
        for (int u = f * 4 + wave; u < 108; u += 96) task_b15(p, u, lane);
    }
    const rsrc_t rw = mk_rsrc(p.ws, p.wsbytes);
    const int nb = f / 6, t = (f / 3) & 1, tg = f % 3;
    for (int s = wave; s < 64; s += 4) {
        const int tile = s * 2 + t;
        if (!wait_fin(p.ws, OFF_CTRL + C_WFL + (unsigned)(((s < FAST0 ? s : FAST0 - 1) * 4 + nb) * 2 + t) * 512, OFF_CTRL + C_BBF + tile * 64, lane, s < FAST0 - 1)) return;
        v2u bb[3];
#pragma unroll
        for (int n = 0; n < 3; ++n) bb[n] = __builtin_amdgcn_raw_buffer_load_b64(rw, OFF_BB + (unsigned)(tile * 36 + nb * 9 + tg * 3 + n) * 512 + lane * 8, 0, 16);
        const float g = __uint_as_float(__builtin_amdgcn_raw_buffer_load_b32(rw, OFF_G + (tile * 16 + (lane & 15)) * 4, 0, 16));
        v4u pv[3][8];
        for (unsigned spins = 0;;) {
#pragma unroll
            for (int n = 0; n < 3; ++n)
#pragma unroll
                for (int kq = 0; kq < 8; ++kq) pv[n][kq] = ld_sc1(rw, OFF_WP + (unsigned)(((s * 4 + nb) * 8 + kq) * 18 + t * 9 + tg * 3 + n) * 1024 + lane * 16);
            __builtin_amdgcn_sched_barrier(0);
            if (s < FAST0) break;
            unsigned m = 0;
#pragma unroll
            for (int n = 0; n < 3; ++n)
#pragma unroll
                for (int kq = 0; kq < 8; ++kq) m = max(m, mx4(pv[n][kq]));
            if (fresh_or_dead(m, spins, p.ws)) break;
        }
#pragma unroll
        for (int n = 0; n < 3; ++n) {
            f32x4 a = __builtin_bit_cast(f32x4, pv[n][0]);
#pragma unroll
            for (int kq = 1; kq < 8; ++kq) a += __builtin_bit_cast(f32x4, pv[n][kq]);
            const float b0 = __uint_as_float(bb[n].x << 16), b1 = __uint_as_float(bb[n].x & 0xffff0000u), b2 = __uint_as_float(bb[n].y << 16), b3 = __uint_as_float(bb[n].y & 0xffff0000u);
            const v2u o = {pk2(rbf(g * rbf(a[0])) + b0, rbf(g * rbf(a[1])) + b1), pk2(rbf(g * rbf(a[2])) + b2, rbf(g * rbf(a[3])) + b3)};
            *(v2u*)(p.out + (size_t)(tile * 16 + (lane & 15)) * 576 + (nb * 9 + tg * 3 + n) * 16 + 4 * (lane >> 4)) = o;
        }
        TL(4, s * 64 + f);
    }
}

__global__ void __launch_bounds__(256, 1) k_main(Params p) {
    extern __shared__ __attribute__((aligned(16))) unsigned char lds[];
    const int b = blockIdx.x, x = b & 7, r = b >> 3;
    const int lane = threadIdx.x & 63, wave = __builtin_amdgcn_readfirstlane(threadIdx.x >> 6);
    TL(0, b * 4 + wave);
    const int slot = __builtin_amdgcn_readfirstlane(b * 4 + wave);
    if (x < 4) {
        if (r < 8) { role_head(p, r * 4 + x, wave, slot); return; }
        if (r < 16) { role_vcu(p, (r - 8) * 4 + x, wave, slot); return; }
        if (b == 251 && wave == 3) { hs_leader(p.ws, slot, lane); return; }
        if (r == 30) {
            const unsigned hc = hs_begin(p.ws, slot);
            const unsigned res = probe_pages(p.ws, x * 4 + wave, lane);
            if (!hs_wait<false>(p.ws, slot, hc)) return;
            __hip_atomic_store((gu32*)(p.ws + OFF_CTRL + C_PRB + (x * 4 + wave) * 4), res, RLX_AGENT);
        } else {
            if (b != 251) prefetch_head_w(p, x, (r - 16) * 4 + wave, lane);
            if (!hs_wait<false>(p.ws, slot, hs_begin(p.ws, slot))) return;
        }
        const int i = (r - 16) * 4 + x;
        if (wave == 0 || (wave == 1 && i < 8)) { const unsigned sel = wait_sel(p.ws); if (sel != ~0u) role_red(p, wave * 64 + i, lane, sel); }
    } else if (r < 16) {
        const int i = r * 4 + (x - 4);
        role_kv(p, lds, i, wave, lane, slot);
        if (i < 32) task_b2(p, lds, i, wave);
        else if (i < 56) role_fin(p, i - 32, wave);
    } else {
        const int i = (r - 16) * 4 + (x - 4);
        const v2u old = hs_peek(p.ws, slot);
        v4u xv[9];
#pragma unroll
        for (int k = 0; k < 9; ++k) xv[k] = *(const v4u*)(p.x + (size_t)i * 32 * 576 + (k * 256 + threadIdx.x) * 8);
        __builtin_amdgcn_sched_barrier(0);
        init_ws(p.ws, p.mem0, i);
        const unsigned hc = hs_challenge(p.ws, slot, old);
        const unsigned n0 = __hip_atomic_load((gu32*)(p.ws + OFF_HS + HS_B), RLX_AGENT);
        asm volatile("s_waitcnt vmcnt(0)" :: "v"(n0) : "memory");
        __hip_atomic_store((gu32*)(p.ws + OFF_HS + HS_R + slot * 4), n0, RLX_AGENT);
        if (threadIdx.x < 64) TL(0, 3100 + i);
        build_xf(p, lds, i, xv);
        DRAIN();
        __syncthreads();
        if (threadIdx.x < 64) TL(6, 2048 + i);
        if (!hs_wait<true>(p.ws, slot, hc, wave == 0 ? i : -1)) return;
        if (!wait_ge(p.ws, OFF_CTRL + C_KVF + (i & 31) * 64, 1)) return;
        poison_fast(p.ws, i);
        if (i >= 32) {
#pragma unroll 1
            for (int task = i - 32; task < 64; task += 32) task_b1(p, lds, task, wave);
        }
        if (!wait_deferred(p.ws, lane)) return;
        if (i < 32) role_att(p, lds, i, wave, lane); else role_wo(p, lds, i - 32, wave, lane);
    }
}

static void launch_all(void* const* d_in, void* d_out, void* d_ws, size_t ws_size, hipStream_t stream, unsigned mask, int do_init) {
    Params p;
    p.x = (const unsigned short*)d_in[0]; p.mem0 = (const unsigned short*)d_in[1]; p.wq = (const unsigned short*)d_in[2]; p.wk = (const unsigned short*)d_in[3];
    p.wv = (const unsigned short*)d_in[4]; p.wo = (const unsigned short*)d_in[5]; p.wqkv = (const unsigned short*)d_in[8]; p.wob = (const unsigned short*)d_in[9];
    p.gw = (const unsigned short*)d_in[10]; p.out = (unsigned short*)d_out;
    unsigned char* base = (unsigned char*)(((uintptr_t)d_ws + (2u << 20) - 1) & ~(uintptr_t)((2u << 20) - 1));
    p.ws = base; p.wsbytes = WS_BYTES; p.mask = mask;
    (void)ws_size;
    (void)hipFuncSetAttribute((const void*)k_main, hipFuncAttributeMaxDynamicSharedMemorySize, LDS_BYTES);
    (void)do_init;
    k_main<<<256, 256, LDS_BYTES, stream>>>(p);
}
extern "C" void kernel_launch(void* const* d_in, const int* in_sizes, int n_in, void* d_out, int out_size, void* d_ws, size_t ws_size, hipStream_t stream) {
    (void)in_sizes; (void)n_in; (void)out_size;
    launch_all(d_in, d_out, d_ws, ws_size, stream, 0xffffffffu, 1);
}
```
